```python
import torch

def _softmax(x, axis=-1):
    return torch.softmax(x, dim=axis)
def _gelu(x):
    return torch.nn.functional.gelu(x, approximate="tanh")
def _silu(x):
    return torch.nn.functional.silu(x)
def _sigmoid(x):
    return torch.sigmoid(x)
def _layernorm(x, axis=-1, eps=1e-5):
    m = x.mean(dim=axis, keepdim=True)
    v = x.var(dim=axis, keepdim=True, unbiased=False)
    return (x - m) / torch.sqrt(v + eps)
def _scatter_add(tgt, idx, src, axis=0):
    return tgt.clone().index_add_(axis, idx, src)
def _segment_sum(src, seg, n):
    out = torch.zeros((n,) + src.shape[1:], dtype=src.dtype)
    return out.index_add_(0, seg, src)
def _pad(x, pw):
    flat = tuple(v for pair in reversed(pw) for v in pair)
    return torch.nn.functional.pad(x, flat)

kernel_name = 'sequential_preLN__mha_block_cached__dense_activation_silu__decode'

def setup_inputs():

    _w = (lambda a: (lambda t: t.to(torch.bfloat16) if t.is_floating_point() else t)(torch.as_tensor(a)))
    def _A_setup():
        B, Sq, D = 4, 1, 448
        H, Sk, Dh = 16, 8192, 256
        return {
            "x": _w((lambda *s: torch.randn(*s, dtype=torch.bfloat16) * 0.05)(B, Sq, D)),
            "k_cache": _w((lambda *s: torch.randn(*s, dtype=torch.bfloat16) * 3.2)(B, H, Sk, Dh)),
            "v_cache": _w((lambda *s: torch.randn(*s, dtype=torch.bfloat16) * 0.8)(B, H, Sk, Dh)),
            "wq": _w((lambda *s: torch.randn(*s, dtype=torch.bfloat16) * 3.2)(D, H * Dh) * (D ** -0.5)),
            "wo": _w((lambda *s: torch.randn(*s, dtype=torch.bfloat16) * 0.8)(H * Dh, D) * ((H * Dh) ** -0.5)),
        }
    _A_in = _A_setup()

    def _B_setup():
        B, S, D, F = 4, 1, 448, 1792
        return {
            "x": _w((lambda *s: torch.randn(*s, dtype=torch.bfloat16) * 0.05)(B, S, D)),
            "w1": _w((lambda *s: torch.randn(*s, dtype=torch.bfloat16) * 3.2)(D, F) * (D ** -0.5)),
            "w2": _w((lambda *s: torch.randn(*s, dtype=torch.bfloat16) * 0.8)(F, D) * (F ** -0.5)),
        }
    _B_in = _B_setup()

    return {
        "A_x": _A_in["x"],
        "A_k_cache": _A_in["k_cache"],
        "A_v_cache": _A_in["v_cache"],
        "A_wq": _A_in["wq"],
        "A_wo": _A_in["wo"],
        "B_x": _B_in["x"],
        "B_w1": _B_in["w1"],
        "B_w2": _B_in["w2"],
    }

def reference(A_x, A_k_cache, A_v_cache, A_wq, A_wo, B_x, B_w1, B_w2):
    def _A_ref(x, k_cache, v_cache, wq, wo):
        B, Sq, D = x.shape
        H, Dh = 16, 256
        q = (lambda x, *d: x.permute(*d))((x @ wq).reshape(B, Sq, H, Dh), 0, 2, 1, 3)
        scores = torch.einsum("bhqd,bhkd->bhqk", q, k_cache) * (Dh ** -0.5)
        p = _softmax(scores, axis=-1)
        o = torch.einsum("bhqk,bhkd->bhqd", p, v_cache)
        return (lambda x, *d: x.permute(*d))(o, 0, 2, 1, 3).reshape(B, Sq, H * Dh) @ wo

    def _B_ref(x, w1, w2):
        h = torch.einsum("bsd,df->bsf", x, w1)
        h = _silu(h)
        out = torch.einsum("bsf,fd->bsd", h, w2)
        return out

    x = A_x
    h = x + _A_ref(_layernorm(x, axis=-1), A_k_cache, A_v_cache, A_wq, A_wo)
    h2 = h + _B_ref(_layernorm(h, axis=-1), B_w1, B_w2)
    return h2
```

```cpp
#include <hip/hip_runtime.h>
#include <hip/hip_bf16.h>
#include <stdint.h>

typedef unsigned v4u __attribute__((ext_vector_type(4)));
typedef float f32x4 __attribute__((ext_vector_type(4)));
typedef __attribute__((ext_vector_type(2))) __bf16 bf16x2;
typedef unsigned short u16;

constexpr int B = 4, D = 448, H = 16, SK = 8192, DH = 256, F = 1792;
constexpr int NSPLIT = 4, KPW = SK / NSPLIT;
#define ROUND_SCORES 1

__device__ __forceinline__ float bf2f(u16 v) { return __builtin_bit_cast(float, (unsigned)v << 16); }
__device__ __forceinline__ float rbf(float f) { return (float)(__bf16)f; }
__device__ __forceinline__ u16 f2bf(float f) { return __builtin_bit_cast(u16, (__bf16)f); }
__device__ __forceinline__ float dot2(unsigned a, unsigned b, float c) {
  return __builtin_amdgcn_fdot2_f32_bf16(__builtin_bit_cast(bf16x2, a), __builtin_bit_cast(bf16x2, b), c, false);
}
template <int CTRL> __device__ __forceinline__ float dpp(float x) {
  return __builtin_bit_cast(float, __builtin_amdgcn_mov_dpp(__builtin_bit_cast(int, x), CTRL, 0xf, 0xf, true));
}
constexpr int XOR1 = 0xB1, XOR2 = 0x4E, XOR7 = 0x141, XOR8 = 0x128;
__device__ __forceinline__ float wave_sum(float x) {
  for (int o = 32; o > 0; o >>= 1) x += __shfl_xor(x, o);
  return x;
}
__device__ __forceinline__ float wave_max(float x) {
  for (int o = 32; o > 0; o >>= 1) x = fmaxf(x, __shfl_xor(x, o));
  return x;
}

__device__ void layernorm_block(const float* row, float* out, int tid, int nthr, float* red) {
  const int lane = tid & 63;
  float s = 0.f;
  for (int i = lane; i < D; i += 64) s += row[i];
  s = wave_sum(s);
  const float mean = s / D;
  float v = 0.f;
  for (int i = lane; i < D; i += 64) { float d = row[i] - mean; v += d * d; }
  v = wave_sum(v) / D;
  const float mb = rbf(mean), vb = rbf(v);
  const float den = rbf(sqrtf(rbf(vb + 1e-5f)));
  for (int i = tid; i < D; i += nthr) out[i] = rbf(rbf(row[i] - mb) / den);
}

__global__ void __launch_bounds__(256) k_q(const u16* x, const u16* wq, u16* q) {
  __shared__ float row[D], xn[D];
  const int b = blockIdx.x / H, h = blockIdx.x % H, tid = threadIdx.x;
  for (int i = tid; i < D; i += 256) row[i] = bf2f(x[b * D + i]);
  __syncthreads();
  layernorm_block(row, xn, tid, 256, nullptr);
  __syncthreads();
  float acc = 0.f;
  const u16* w = wq + h * DH + tid;
#pragma unroll 16
  for (int d = 0; d < D; ++d) acc = fmaf(xn[d], bf2f(w[(size_t)d * (H * DH)]), acc);
  q[(b * H + h) * DH + tid] = f2bf(acc);
}

__global__ void __launch_bounds__(512) k_attn(const u16* __restrict__ q, const u16* __restrict__ K, const u16* __restrict__ V, float* __restrict__ part) {
  __shared__ __attribute__((aligned(16))) float lds_p[8 * 256];
  __shared__ __attribute__((aligned(16))) float lds_o[8 * 256];
  __shared__ float lds_ml[16];
  const int tid = threadIdx.x, lane = tid & 63, wave = __builtin_amdgcn_readfirstlane(tid >> 6);
  const int bh = blockIdx.x / NSPLIT;
  const int half = lane >> 5, c = lane & 31;
  const v4u qv = *reinterpret_cast<const v4u*>(q + bh * DH + c * 8);
  const size_t base = (size_t)blockIdx.x * KPW * DH;
  const __amdgpu_buffer_rsrc_t rk = __builtin_amdgcn_make_buffer_rsrc((void*)(K + base), 0, KPW * DH * 2, 0x00020000);
  const __amdgpu_buffer_rsrc_t rv = __builtin_amdgcn_make_buffer_rsrc((void*)(V + base), 0, KPW * DH * 2, 0x00020000);
  const int voff = tid * 16;
  constexpr int NL = KPW / 16;
  constexpr int W = 4, R = 8;
  v4u buf[R];
#pragma unroll
  for (int i = 0; i < W; ++i) buf[i] = __builtin_amdgcn_raw_buffer_load_b128(rk, voff, i * 8192, 2);
  float sc[4];
#pragma unroll 1
  for (int g = 0; g < 4; ++g) {
    float x[32];
    const __amdgpu_buffer_rsrc_t rn = g < 3 ? rk : rv;
    const int offn = g < 3 ? (g + 1) * 32 * 8192 : 0;
#pragma unroll
    for (int i = 0; i < 32; ++i) {
      asm volatile("" :: "v"(buf[i % R]) : "memory");
      if (i + W < 32) buf[(i + W) % R] = __builtin_amdgcn_raw_buffer_load_b128(rk, voff, (g * 32 + i + W) * 8192, 2);
      else buf[(i + W) % R] = __builtin_amdgcn_raw_buffer_load_b128(rn, voff, offn + (i + W - 32) * 8192, 2);
      asm volatile("" ::: "memory");
      float d = dot2(buf[i % R][0], qv[0], 0.f);
      d = dot2(buf[i % R][1], qv[1], d);
      d = dot2(buf[i % R][2], qv[2], d);
      d = dot2(buf[i % R][3], qv[3], d);
      asm volatile("" : "+v"(d));
      x[i] = d;
    }
#pragma unroll
    for (int i = 0; i < 16; ++i) {
      auto s = __builtin_amdgcn_permlane16_swap(__builtin_bit_cast(unsigned, x[i]), __builtin_bit_cast(unsigned, x[i + 16]), false, false);
      x[i] = __uint_as_float(s[0]) + __uint_as_float(s[1]);
    }
#define TR_STEP(HALF, CTRL, BIT) _Pragma("unroll") for (int hh = 0; hh < (HALF); ++hh) { \
      const float lo_ = x[hh], hi_ = x[hh + (HALF)]; const float t1_ = lo_ + dpp<CTRL>(lo_), t2_ = hi_ + dpp<CTRL>(hi_); \
      x[hh] = (c & (BIT)) ? t2_ : t1_; }
    TR_STEP(8, XOR8, 8) TR_STEP(4, XOR7, 4) TR_STEP(2, XOR2, 2) TR_STEP(1, XOR1, 1)
#undef TR_STEP
    const float s = x[0];
    if (g == 0) sc[0] = s; else if (g == 1) sc[1] = s; else if (g == 2) sc[2] = s; else sc[3] = s;
  }
  constexpr float LOG2E = 1.4426950408889634f;
  float m = -INFINITY;
#pragma unroll
  for (int g = 0; g < 4; ++g) { sc[g] = (ROUND_SCORES ? rbf(sc[g]) : sc[g]) * 0.0625f; m = fmaxf(m, sc[g]); }
  m = wave_max(m);
  float l = 0.f;
  float* pw = lds_p + wave * 256 + half * 128;
#pragma unroll
  for (int g = 0; g < 4; ++g) { const float p = __builtin_amdgcn_exp2f((sc[g] - m) * LOG2E); l += p; pw[g * 32 + c] = p; }
  l = wave_sum(l);
  asm volatile("s_waitcnt lgkmcnt(0)" ::: "memory");
  float acc[8];
#pragma unroll
  for (int k = 0; k < 8; ++k) acc[k] = 0.f;
#pragma unroll 1
  for (int g = 0; g < NL / 8; ++g) {
    const f32x4 pa = *reinterpret_cast<const f32x4*>(pw + g * 8), pb = *reinterpret_cast<const f32x4*>(pw + g * 8 + 4);
#pragma unroll
    for (int i = 0; i < 8; ++i) {
      asm volatile("" :: "v"(buf[i % R]) : "memory");
      buf[(i + W) % R] = __builtin_amdgcn_raw_buffer_load_b128(rv, voff, (g * 8 + i + W) * 8192, 2);
      asm volatile("" ::: "memory");
      const float p = i < 4 ? pa[i] : pb[i - 4];
#pragma unroll
      for (int k = 0; k < 4; ++k) {
        acc[2 * k] = fmaf(p, __builtin_bit_cast(float, buf[i % R][k] << 16), acc[2 * k]);
        acc[2 * k + 1] = fmaf(p, __builtin_bit_cast(float, buf[i % R][k] & 0xffff0000u), acc[2 * k + 1]);
      }
      asm volatile("" : "+v"(acc[0]), "+v"(acc[1]), "+v"(acc[2]), "+v"(acc[3]), "+v"(acc[4]), "+v"(acc[5]), "+v"(acc[6]), "+v"(acc[7]));
    }
  }
#pragma unroll
  for (int k = 0; k < 8; ++k) acc[k] += __shfl_xor(acc[k], 32);
  if (half == 0) {
    *reinterpret_cast<f32x4*>(lds_o + wave * 256 + c * 8) = f32x4{acc[0], acc[1], acc[2], acc[3]};
    *reinterpret_cast<f32x4*>(lds_o + wave * 256 + c * 8 + 4) = f32x4{acc[4], acc[5], acc[6], acc[7]};
  }
  if (lane == 0) { lds_ml[2 * wave] = m; lds_ml[2 * wave + 1] = l; }
  __syncthreads();
  if (tid < 256) {
    float M = -INFINITY;
#pragma unroll
    for (int w = 0; w < 8; ++w) M = fmaxf(M, lds_ml[2 * w]);
    float L = 0.f, o = 0.f;
#pragma unroll
    for (int w = 0; w < 8; ++w) { const float e = __builtin_amdgcn_exp2f((lds_ml[2 * w] - M) * LOG2E); L += e * lds_ml[2 * w + 1]; o += e * lds_o[w * 256 + tid]; }
    float* pp = part + (size_t)blockIdx.x * 260;
    pp[4 + tid] = o;
    if (tid == 0) { pp[0] = M; pp[1] = L; }
  }
}

__global__ void __launch_bounds__(448) k_oproj(const float* __restrict__ part, const u16* __restrict__ wo, float* __restrict__ ypart) {
  __shared__ float o[B][DH];
  const int h = blockIdx.x / 4, qd = blockIdx.x % 4, tid = threadIdx.x;
  constexpr float LOG2E = 1.4426950408889634f;
  for (int i = tid; i < B * DH; i += 448) {
    const int b = i / DH, d = i % DH;
    const float* pp = part + (size_t)((b * H + h) * NSPLIT) * 260;
    float M = -INFINITY;
    for (int s = 0; s < NSPLIT; ++s) M = fmaxf(M, pp[s * 260]);
    float L = 0.f, a = 0.f;
    for (int s = 0; s < NSPLIT; ++s) { const float e = __builtin_amdgcn_exp2f((pp[s * 260] - M) * LOG2E); L += e * pp[s * 260 + 1]; a += e * pp[s * 260 + 4 + d]; }
    o[b][d] = rbf(a / L);
  }
  __syncthreads();
  float acc[B] = {0.f, 0.f, 0.f, 0.f};
#pragma unroll 16
  for (int d = qd * 64; d < qd * 64 + 64; ++d) {
    const float w = bf2f(wo[(size_t)(h * DH + d) * D + tid]);
#pragma unroll
    for (int b = 0; b < B; ++b) acc[b] = fmaf(o[b][d], w, acc[b]);
  }
#pragma unroll
  for (int b = 0; b < B; ++b) ypart[((size_t)blockIdx.x * B + b) * D + tid] = acc[b];
}

__global__ void __launch_bounds__(448) k_mlp(const u16* __restrict__ x, const float* __restrict__ ypart, const u16* __restrict__ w1, const u16* __restrict__ w2,
                                             float* __restrict__ hbuf, float* __restrict__ opart) {
  __shared__ float hrow[B][D], hn[B][D], h1[B][64], red[7][B][64];
  const int tid = threadIdx.x, f0 = blockIdx.x * 64;
  for (int b = 0; b < B; ++b) {
    float s = 0.f;
#pragma unroll 16
    for (int p = 0; p < 64; ++p) s += ypart[((size_t)p * B + b) * D + tid];
    const float hv = rbf(bf2f(x[b * D + tid]) + rbf(s));
    hrow[b][tid] = hv;
    if (blockIdx.x == 0) hbuf[b * D + tid] = hv;
  }
  __syncthreads();
  for (int b = 0; b < B; ++b) layernorm_block(hrow[b], hn[b], tid, 448, nullptr);
  __syncthreads();
  {
    const int col = tid & 63, seg = tid >> 6;
    float acc[B] = {0.f, 0.f, 0.f, 0.f};
#pragma unroll 16
    for (int d = seg * 64; d < seg * 64 + 64; ++d) {
      const float w = bf2f(w1[(size_t)d * F + f0 + col]);
#pragma unroll
      for (int b = 0; b < B; ++b) acc[b] = fmaf(hn[b][d], w, acc[b]);
    }
#pragma unroll
    for (int b = 0; b < B; ++b) red[seg][b][col] = acc[b];
  }
  __syncthreads();
  if (tid < 256) {
    const int b = tid >> 6, col = tid & 63;
    float s = 0.f;
    for (int g = 0; g < 7; ++g) s += red[g][b][col];
    s = rbf(s);
    h1[b][col] = rbf(s / (1.f + __expf(-s)));
  }
  __syncthreads();
  float acc[B] = {0.f, 0.f, 0.f, 0.f};
#pragma unroll 16
  for (int f = 0; f < 64; ++f) {
    const float w = bf2f(w2[(size_t)(f0 + f) * D + tid]);
#pragma unroll
    for (int b = 0; b < B; ++b) acc[b] = fmaf(h1[b][f], w, acc[b]);
  }
#pragma unroll
  for (int b = 0; b < B; ++b) opart[((size_t)blockIdx.x * B + b) * D + tid] = acc[b];
}

__global__ void __launch_bounds__(448) k_final(const float* __restrict__ hbuf, const float* __restrict__ opart, u16* __restrict__ out) {
  const int b = blockIdx.x, tid = threadIdx.x;
  float s = 0.f;
#pragma unroll
  for (int p = 0; p < 28; ++p) s += opart[((size_t)p * B + b) * D + tid];
  out[b * D + tid] = f2bf(hbuf[b * D + tid] + rbf(s));
}

extern "C" void kernel_launch(void* const* d_in, const int* in_sizes, int n_in, void* d_out, int out_size, void* d_ws, size_t ws_size, hipStream_t stream) {
  const u16* x = (const u16*)d_in[0];
  const u16* K = (const u16*)d_in[1];
  const u16* V = (const u16*)d_in[2];
  const u16* wq = (const u16*)d_in[3];
  const u16* wo = (const u16*)d_in[4];
  const u16* w1 = (const u16*)d_in[6];
  const u16* w2 = (const u16*)d_in[7];
  char* ws = (char*)d_ws;
  u16* q = (u16*)ws;
  float* part = (float*)(ws + (64 << 10));
  float* ypart = (float*)(ws + (1 << 20));
  float* hbuf = (float*)(ws + (2 << 20));
  float* opart = (float*)(ws + (3 << 20));
  k_q<<<64, 256, 0, stream>>>(x, wq, q);
  k_attn<<<256, 512, 0, stream>>>(q, K, V, part);
  k_oproj<<<64, 448, 0, stream>>>(part, wo, ypart);
  k_mlp<<<28, 448, 0, stream>>>(x, ypart, w1, w2, hbuf, opart);
  k_final<<<4, 448, 0, stream>>>(hbuf, opart, (u16*)d_out);
}
```

```cpp
#include <hip/hip_runtime.h>
#include <stdint.h>

typedef unsigned v4u __attribute__((ext_vector_type(4)));
typedef unsigned v2u __attribute__((ext_vector_type(2)));
typedef float f32x4 __attribute__((ext_vector_type(4)));
typedef float f32x2 __attribute__((ext_vector_type(2)));
typedef __bf16 bf16x2 __attribute__((ext_vector_type(2)));
typedef __bf16 bf16x4 __attribute__((ext_vector_type(4)));
typedef __bf16 bf16x8 __attribute__((ext_vector_type(8)));
typedef short s16x4 __attribute__((ext_vector_type(4)));
typedef unsigned short u16;
typedef unsigned long long u64;
#define LAS __attribute__((address_space(3)))

constexpr int NB = 4, DM = 448, NH = 16, SK = 8192, DH = 256, FF = 1792;
constexpr int NWG = 256, NTHR = 512;
constexpr int NL = 256;
#define WINDOW 6
constexpr int WIN = WINDOW;
constexpr int NUNIT = 56;
constexpr int NM = 112;
#define HREP 4
constexpr unsigned SENT = 0xFFFFFFFFu;
constexpr int AUX_NT = 2, AUX_SC1 = 16;
#define AUX_WQ 2
#define AUX_WO 2
#define MAPPING 1
#define NO_PREP 0
#define NO_QWAIT 0
#define CODE_PF 1
#define PF_AT 13
#define PREP_SLEEP 100
#define F_SLEEP 24
constexpr unsigned OOB = 0x7FFFFFF0u;
constexpr float LOG2E = 1.4426950408889634f;

constexpr unsigned WS_EPOCH = 0, WS_READY = 4096, WS_ML = 8192, WS_HB = 16384, WS_Q = 1u << 17, WS_Y = 1u << 18, WS_O = 1u << 20, WS_BYTES = 2u << 20;
constexpr unsigned WS_STAMP = 64u << 20;
constexpr int L_WOF = 0;
constexpr int L_W1 = L_WOF + 3 * 32 * 1024;
constexpr int L_W2 = L_W1 + 14336;
constexpr int L_SC = L_W2 + 14336;
constexpr int L_QB = L_SC + 8192;
constexpr int L_OL = L_QB + 512;
constexpr int OL_STRIDE = 528;
constexpr int L_HN = L_OL + 16 * OL_STRIDE;
constexpr int HN_STRIDE = 912;
constexpr int L_MISC = L_HN + 4 * HN_STRIDE;
constexpr int L_BYTES = L_MISC + 128;
static_assert(L_BYTES <= 163840 && L_MISC % 16 == 0 && L_HN % 16 == 0, "lds");

__device__ __forceinline__ float u2f(unsigned u) { return __builtin_bit_cast(float, u); }
__device__ __forceinline__ unsigned f2u(float f) { return __builtin_bit_cast(unsigned, f); }
__device__ __forceinline__ float rbf(float f) { return (float)(__bf16)f; }
__device__ __forceinline__ unsigned pk2(float lo, float hi) { bf16x2 v = {(__bf16)lo, (__bf16)hi}; return __builtin_bit_cast(unsigned, v); }
__device__ __forceinline__ float blo(unsigned u) { return u2f(u << 16); }
__device__ __forceinline__ float bhi(unsigned u) { return u2f(u & 0xffff0000u); }
__device__ __forceinline__ float dot2(unsigned a, unsigned b, float c) {
  return __builtin_amdgcn_fdot2_f32_bf16(__builtin_bit_cast(bf16x2, a), __builtin_bit_cast(bf16x2, b), c, false);
}
template <int CTRL> __device__ __forceinline__ float dpp(float x) {
  return __builtin_bit_cast(float, __builtin_amdgcn_mov_dpp(__builtin_bit_cast(int, x), CTRL, 0xf, 0xf, true));
}
constexpr int XOR1 = 0xB1, XOR2 = 0x4E, XOR7 = 0x141, ROR8 = 0x128, ROR4 = 0x124;
__device__ __forceinline__ float swap16_add(float a, float b) {
  auto s = __builtin_amdgcn_permlane16_swap(f2u(a), f2u(b), false, false);
  const unsigned s0 = s[0], s1 = s[1];
  return u2f(s0) + u2f(s1);
}
__device__ __forceinline__ float swap32_add(float a, float b) {
  auto s = __builtin_amdgcn_permlane32_swap(f2u(a), f2u(b), false, false);
  const unsigned s0 = s[0], s1 = s[1];
  return u2f(s0) + u2f(s1);
}
__device__ __forceinline__ float swap16_max(float a) {
  auto s = __builtin_amdgcn_permlane16_swap(f2u(a), f2u(a), false, false);
  const unsigned s0 = s[0], s1 = s[1];
  return fmaxf(u2f(s0), u2f(s1));
}
__device__ __forceinline__ float swap32_max(float a) {
  auto s = __builtin_amdgcn_permlane32_swap(f2u(a), f2u(a), false, false);
  const unsigned s0 = s[0], s1 = s[1];
  return fmaxf(u2f(s0), u2f(s1));
}
__device__ __forceinline__ float row_sum(float x) { x += dpp<ROR8>(x); x += dpp<ROR4>(x); x += dpp<XOR2>(x); x += dpp<XOR1>(x); return x; }
__device__ __forceinline__ float row_max(float x) { x = fmaxf(x, dpp<ROR8>(x)); x = fmaxf(x, dpp<ROR4>(x)); x = fmaxf(x, dpp<XOR2>(x)); x = fmaxf(x, dpp<XOR1>(x)); return x; }
__device__ __forceinline__ float wave_sum(float x) { x = row_sum(x); x = swap16_add(x, x); return swap32_add(x, x); }
__device__ __forceinline__ float wave_max(float x) { x = row_max(x); x = swap16_max(x); return swap32_max(x); }
__device__ __forceinline__ unsigned nosent(float f) { const unsigned u = f2u(f); return u < 0xFFFFFFFEu ? u : 0xFFFFFFFEu; }

__device__ __forceinline__ __amdgpu_buffer_rsrc_t mkrs(const void* p, unsigned bytes) { return __builtin_amdgcn_make_buffer_rsrc((void*)p, 0, bytes, 0x00020000); }
#define LD128(rs, vo, so, aux) __builtin_amdgcn_raw_buffer_load_b128(rs, vo, so, aux)
#define LD64(rs, vo, so, aux) __builtin_amdgcn_raw_buffer_load_b64(rs, vo, so, aux)
#define ST128(v, rs, vo, so, aux) __builtin_amdgcn_raw_buffer_store_b128(v, rs, vo, so, aux)
#define ST64(v, rs, vo, so, aux) __builtin_amdgcn_raw_buffer_store_b64(v, rs, vo, so, aux)
#define ST32(v, rs, vo, so, aux) __builtin_amdgcn_raw_buffer_store_b32(v, rs, vo, so, aux)
#define PIN() asm volatile("" ::: "memory")
#define USE(x) asm volatile("" :: "v"(x) : "memory")
#define REDEF(x) asm volatile("" : "+v"(x) :: "memory")
#define BARRIER() asm volatile("s_waitcnt lgkmcnt(0)\n\ts_barrier" ::: "memory")
#define DRAIN() asm volatile("s_waitcnt vmcnt(0)" ::: "memory")
#define LGKM0() asm volatile("s_waitcnt lgkmcnt(0)" ::: "memory")
constexpr int SPIN_CAP = 1 << 16;
constexpr int LSPIN_CAP = 1 << 20;

#define STAMP(i) do {} while (0)

__device__ __forceinline__ void ln8(float (&v)[8], int lane) {
  float s = ((v[0] + v[1]) + (v[2] + v[3])) + ((v[4] + v[5]) + (v[6] + v[7]));
  const float mean = wave_sum(s) * (1.0f / DM);
  float q = 0.f;
#pragma unroll
  for (int e = 0; e < 8; ++e) { const float d = v[e] - mean; q = fmaf(d, d, q); }
  q = lane < 56 ? q : 0.f;
  const float var = wave_sum(q) * (1.0f / DM);
  const float mb = rbf(mean);
  const float den = rbf(__builtin_amdgcn_sqrtf(rbf(rbf(var) + 1e-5f)));
  const float inv = __builtin_amdgcn_rcpf(den);
#pragma unroll
  for (int e = 0; e < 8; ++e) v[e] = rbf(v[e] - mb) * inv;
}
__device__ __forceinline__ float reduce_pieces(f32x4 v) {
  const float a = swap32_add(v[0], v[2]), b = swap32_add(v[1], v[3]);
  float c = swap16_add(a, b);
  c += dpp<ROR8>(c); c += dpp<ROR4>(c); c += dpp<XOR2>(c);
  return c;
}
__device__ __forceinline__ bool valid4(v4u a) { const unsigned m0 = a[0] > a[1] ? a[0] : a[1], m1 = a[2] > a[3] ? a[2] : a[3]; return (m0 > m1 ? m0 : m1) != SENT; }
__device__ __forceinline__ f32x4 asf(v4u a) { return __builtin_bit_cast(f32x4, a); }

#define SPIN(cap) _Pragma("unroll 1") for (int spin = 0; spin < (cap); ++spin)
#define LDS_INC(w) __hip_atomic_fetch_add((LAS unsigned*)(smem + L_MISC) + (w), 1u, __ATOMIC_RELAXED, __HIP_MEMORY_SCOPE_WORKGROUP)

__global__ void __launch_bounds__(NTHR, 2)
fused_decode(const u16* x, const u16* Kc, const u16* Vc, const u16* wq, const u16* wo, const u16* w1, const u16* w2, u16* out, unsigned char* ws) {
  __shared__ __attribute__((aligned(16))) unsigned char smem[L_BYTES];
  const int tid = threadIdx.x, lane = tid & 63;
  int wave = __builtin_amdgcn_readfirstlane(tid >> 6);
  const int g = ((blockIdx.x >> 6) << 6) | (((blockIdx.x & 7) + 8 * ((blockIdx.x >> 3) & 1)) << 2) | ((blockIdx.x >> 4) & 3);
  const int b = g >> 6, h = (g >> 2) & 15;
  const int half = lane >> 5, c32 = lane & 31, g4 = lane >> 4, c16 = lane & 15;
  volatile LAS unsigned* misc = (volatile LAS unsigned*)(smem + L_MISC);
  const __amdgpu_buffer_rsrc_t rws = mkrs(ws, WS_BYTES);
  const __amdgpu_buffer_rsrc_t rk = mkrs(Kc + (size_t)g * (2048 * DH), 2048 * DH * 2);
  const __amdgpu_buffer_rsrc_t rx = mkrs(x, NB * DM * 2);
  const int voff = tid * 16;
  const int hw = wave - 4;
  v4u kb[32];
  u64 pc0;
  asm volatile("s_getpc_b64 %0\n.Lfd_code_begin:" : "=s"(pc0));
  STAMP(0);
  if (wave < 4) {
#pragma unroll
    for (int i = 0; i < WIN; ++i) kb[i] = LD128(rk, voff, i * 4096, AUX_NT);
  } else {
    const int j = b * 4 + (g & 3);
    const __amdgpu_buffer_rsrc_t rq = mkrs(wq + h * DH + j * 16, (DM - 1) * (NH * DH * 2) + 32);
    kb[0] = LD128(rx, lane < 56 ? lane * 16 : (int)OOB, hw * (DM * 2), 0);
#pragma unroll
    for (int i = 0; i < 4; ++i) kb[1 + i] = LD128(rq, (lane >> 1) * (NH * DH * 2) + (lane & 1) * 16, (hw + 4 * i) * 32 * (NH * DH * 2), AUX_WQ);
    kb[5] = LD128(rws, 0, WS_EPOCH, AUX_SC1);
#pragma unroll
    for (int i = 6; i < WIN; ++i) asm volatile("" : "=v"(kb[i]));
    if (tid == 256) { misc[12] = 0u; misc[13] = 0u; misc[14] = 0u; misc[15] = 0u; misc[16] = 0u; }
  }
  BARRIER();
  asm volatile("" : "+s"(wave));

  const int unit = g & 63;
  const bool is_m = (g & 15) < 7;
  const int mi = (g >> 4) * 7 + (g & 15);
  v4u wf[32];
  {
    const int trole = wave;
    if (wave < 4) {
      const __amdgpu_buffer_rsrc_t rv = mkrs(Vc + (size_t)g * (2048 * DH), 2048 * DH * 2);
#pragma unroll
      for (int i = WIN; i < 32; ++i) { USE(kb[i - WIN]); kb[i] = LD128(rk, voff, i * 4096, AUX_NT); PIN(); }
      STAMP(1);
      BARRIER();
      STAMP(2);
      const v4u qv = *(const v4u*)(smem + L_QB + c32 * 16);
      float* sc = (float*)(smem + L_SC) + (wave * 2 + half) * 256;
      float m = -INFINITY;
#define DOT(i) do { float d_ = dot2(kb[i][0], qv[0], 0.f); d_ = dot2(kb[i][1], qv[1], d_); d_ = dot2(kb[i][2], qv[2], d_); d_ = dot2(kb[i][3], qv[3], d_); \
                    asm volatile("" : "+v"(d_)); xs[i] = d_; } while (0)
#define TR_STEP(HALF, CTRL, BIT) _Pragma("unroll") for (int hh = 0; hh < (HALF); ++hh) { \
      const float lo_ = xs[hh], hi_ = xs[hh + (HALF)]; const float t1_ = lo_ + dpp<CTRL>(lo_), t2_ = hi_ + dpp<CTRL>(hi_); \
      xs[hh] = (c32 & (BIT)) ? t2_ : t1_; }
#define REDUCE(grp) do { \
      _Pragma("unroll") for (int i = 0; i < 16; ++i) xs[i] = swap16_add(xs[i], xs[i + 16]); \
      TR_STEP(8, ROR8, 8) TR_STEP(4, XOR7, 4) TR_STEP(2, XOR2, 2) TR_STEP(1, XOR1, 1) \
      const float s_ = rbf(xs[0]) * 0.0625f; m = fmaxf(m, s_); sc[(grp) * 32 + c32] = s_; } while (0)
      {
        float xs[32];
#pragma unroll
        for (int i = 0; i < 32; ++i) {
          REDEF(kb[i]);
          DOT(i);
          if (i < WIN) { PIN(); kb[i] = LD128(rk, voff, (32 + i) * 4096, AUX_NT); PIN(); }
        }
        REDUCE(0);
      }
#pragma unroll 1
      for (int gi = 1; gi < 8; ++gi) {
        float xs[32];
        const __amdgpu_buffer_rsrc_t rn = gi < 7 ? rk : rv;
        const int offn = gi < 7 ? (gi + 1) * 32 * 4096 : 0;
#pragma unroll
        for (int i = 0; i < 32; ++i) {
          USE(kb[i]);
          if (i + WIN < 32) kb[i + WIN] = LD128(rk, voff, (gi * 32 + i + WIN) * 4096, AUX_NT);
          else kb[i + WIN - 32] = LD128(rn, voff, offn + (i + WIN - 32) * 4096, AUX_NT);
          REDEF(kb[i]);
          DOT(i);
        }
        REDUCE(gi);
      }
      STAMP(3);
      m = wave_max(m);
      {
        LGKM0();
        f32x4* ps = (f32x4*)(smem + L_SC) + wave * 128 + lane * 2;
        f32x4 a = ps[0], bq = ps[1];
        const float mm = m * LOG2E;
#pragma unroll
        for (int e = 0; e < 4; ++e) { a[e] = __builtin_amdgcn_exp2f(fmaf(a[e], LOG2E, -mm)); bq[e] = __builtin_amdgcn_exp2f(fmaf(bq[e], LOG2E, -mm)); }
        ps[0] = a; ps[1] = bq;
        float l = ((a[0] + a[1]) + (a[2] + a[3])) + ((bq[0] + bq[1]) + (bq[2] + bq[3]));
        l = wave_sum(l);
        if (lane == 0) { misc[2 * wave] = f2u(m); misc[2 * wave + 1] = f2u(l); }
        LGKM0();
        if (lane == 0) LDS_INC(12);
      }
      float acc[8];
#pragma unroll
      for (int k = 0; k < 8; ++k) acc[k] = 0.f;
#pragma unroll 1
      for (int gi = 0; gi < NL / 16; ++gi) {
        f32x4 p4[4];
#pragma unroll
        for (int i = 0; i < 4; ++i) p4[i] = *(const f32x4*)(sc + gi * 16 + i * 4);
        if (gi == PF_AT && tid == 0) misc[16] = 1u;
#pragma unroll
        for (int i = 0; i < 16; ++i) {
          USE(kb[i]);
          kb[(i + WIN) & 15] = LD128(rv, voff, (gi * 16 + i + WIN) * 4096, AUX_NT);
          REDEF(kb[i]);
          const float p = p4[i >> 2][i & 3];
#pragma unroll
          for (int k = 0; k < 4; ++k) { acc[2 * k] = fmaf(p, blo(kb[i][k]), acc[2 * k]); acc[2 * k + 1] = fmaf(p, bhi(kb[i][k]), acc[2 * k + 1]); }
          asm volatile("" : "+v"(acc[0]), "+v"(acc[1]), "+v"(acc[2]), "+v"(acc[3]), "+v"(acc[4]), "+v"(acc[5]), "+v"(acc[6]), "+v"(acc[7]));
        }
      }
      STAMP(4);
      {
        v4u hi, lo;
#pragma unroll
        for (int k = 0; k < 4; ++k) {
          hi[k] = pk2(acc[2 * k], acc[2 * k + 1]);
          lo[k] = pk2(acc[2 * k] - blo(hi[k]), acc[2 * k + 1] - bhi(hi[k]));
        }
        unsigned char* ol = smem + L_OL + (wave * 4 + half * 2) * OL_STRIDE + c32 * 16;
        *(v4u*)ol = hi; *(v4u*)(ol + OL_STRIDE) = lo;
      }
      BARRIER();
      if (wave < 3) {
        const v4u* src = (const v4u*)(smem + L_WOF + wave * 32768) + lane;
#pragma unroll
        for (int i = 0; i < 32; ++i) wf[i] = src[i * 64];
      }
    } else {
      {
        float v[8];
#pragma unroll
        for (int k = 0; k < 4; ++k) { v[2 * k] = blo(kb[0][k]); v[2 * k + 1] = bhi(kb[0][k]); }
        ln8(v, lane);
        const v4u xn = {pk2(v[0], v[1]), pk2(v[2], v[3]), pk2(v[4], v[5]), pk2(v[6], v[7])};
        if (lane < 56) *(v4u*)(smem + L_HN + hw * HN_STRIDE + lane * 16) = xn;
#pragma unroll
        for (int i = 0; i < 4; ++i) if (hw + 4 * i < 14) *(v4u*)(smem + L_W1 + (hw + 4 * i) * 1024 + lane * 16) = kb[1 + i];
        LGKM0();
        if (lane == 0) LDS_INC(15);
      }
      const unsigned e0 = __builtin_amdgcn_readfirstlane(kb[5][0]), e1 = __builtin_amdgcn_readfirstlane(kb[5][1]);
      v2u tagv, nextep;
      unsigned qtag = e0 + 0x9E3779B9u;
      qtag ^= qtag >> 16; qtag *= 0x85EBCA6Bu; qtag ^= qtag >> 13; qtag *= 0xC2B2AE35u; qtag ^= qtag >> 16;
      {
        u64 tag = ((u64)e1 << 32) | e0;
        const u64 ne = tag + 1;
        tag += 0x9E3779B97F4A7C15ull; tag = (tag ^ (tag >> 30)) * 0xBF58476D1CE4E5B9ull; tag = (tag ^ (tag >> 27)) * 0x94D049BB133111EBull; tag ^= tag >> 31;
        tagv = v2u{(unsigned)tag, (unsigned)(tag >> 32)};
        nextep = v2u{(unsigned)ne, (unsigned)(ne >> 32)};
      }
      if (hw == 0) {
        SPIN(LSPIN_CAP) { PIN(); if (misc[15] >= 4u) break; }
        PIN();
        const int q = c16 >> 2, p = c16 & 3;
        const LAS unsigned char* s1 = (const LAS unsigned char*)(smem + L_W1) + (g4 * 8 + q) * 32 + p * 8;
        const unsigned char* hp = smem + L_HN + (c16 & 3) * HN_STRIDE + g4 * 16;
        bf16x8 a1[14], bfr[14];
#pragma unroll
        for (int s = 0; s < 14; ++s) {
          const bf16x4 lo = __builtin_amdgcn_ds_read_tr16_b64_v4bf16((LAS bf16x4*)(s1 + s * 1024));
          const bf16x4 hi = __builtin_amdgcn_ds_read_tr16_b64_v4bf16((LAS bf16x4*)(s1 + s * 1024 + 128));
          a1[s] = __builtin_shufflevector(lo, hi, 0, 1, 2, 3, 4, 5, 6, 7);
          bfr[s] = *(const bf16x8*)(hp + s * 64);
        }
        __builtin_amdgcn_sched_barrier(0);
        f32x4 da = {0.f, 0.f, 0.f, 0.f}, db = {0.f, 0.f, 0.f, 0.f};
#pragma unroll
        for (int s = 0; s < 14; s += 2) {
          da = __builtin_amdgcn_mfma_f32_16x16x32_bf16(a1[s], bfr[s], da, 0, 0, 0);
          db = __builtin_amdgcn_mfma_f32_16x16x32_bf16(a1[s + 1], bfr[s + 1], db, 0, 0, 0);
        }
        da += db;
        const v4u qg = {pk2(da[0], da[1]), qtag, pk2(da[2], da[3]), qtag};
        ST128(qg, rws, c16 < 4 ? c16 * 1024 + g4 * 16 : (int)OOB, WS_Q + h * 4096 + (b * 4 + (g & 3)) * 64, AUX_SC1);
        STAMP(1);
      } else if (hw == 1) {
        v4u qg;
        SPIN(SPIN_CAP) {
          PIN();
          qg = LD128(rws, lane * 16, WS_Q + h * 4096 + b * 1024, AUX_SC1);
          if (NO_QWAIT || __all(qg[1] == qtag && qg[3] == qtag)) break;
        }
        *(v2u*)(smem + L_QB + lane * 8) = v2u{qg[0], qg[2]};
        STAMP(1);
      }
      BARRIER();
      if (hw == 0) {
        const v4u s4 = {SENT, SENT, SENT, SENT};
        if (unit < NUNIT) {
          const unsigned yb = WS_Y + (unsigned)(b * NUNIT + unit) * 2048u, ob = WS_O + (unsigned)(b * NUNIT + unit) * (NM * 32u);
          ST128(s4, rws, lane * 16, yb, AUX_SC1); ST128(s4, rws, lane * 16, yb + 1024, AUX_SC1);
          ST128(s4, rws, lane * 16, ob, AUX_SC1); ST128(s4, rws, lane * 16, ob + 1024, AUX_SC1); ST128(s4, rws, lane * 16, ob + 2048, AUX_SC1);
          ST128(s4, rws, lane < 32 ? lane * 16 : (int)OOB, ob + 3072, AUX_SC1);
          ST128(s4, rws, lane < 2 * HREP ? (lane >> 1) * (NB * DM * 4) + (lane & 1) * 16 : (int)OOB, WS_HB + (b * DM + unit * 8) * 4, AUX_SC1);
        }
        ST128(s4, rws, lane < 2 ? lane * 16 : (int)OOB, WS_ML + g * 32, AUX_SC1);
        DRAIN();
        ST64(tagv, rws, lane == 0 ? 0 : (int)OOB, WS_READY + g * 8, AUX_SC1);
      }
      if (is_m && !NO_PREP) {
        const __amdgpu_buffer_rsrc_t r1 = mkrs(w1 + mi * 16, (DM - 1) * (FF * 2) + 32);
        const __amdgpu_buffer_rsrc_t r2 = mkrs(w2 + (size_t)mi * 16 * DM, 16 * DM * 2);
        v4u t1[4], t2[4];
#pragma unroll
        for (int i = 0; i < 4; ++i) {
          t1[i] = LD128(r1, (lane >> 1) * (FF * 2) + (lane & 1) * 16, (hw + 4 * i) * 32 * (FF * 2), AUX_NT);
          t2[i] = LD128(r2, lane * 16, (hw + 4 * i) * 1024, AUX_NT);
        }
#pragma unroll
        for (int i = 0; i < 4; ++i) if (hw + 4 * i < 14) {
          *(v4u*)(smem + L_W1 + (hw + 4 * i) * 1024 + lane * 16) = t1[i];
          *(v4u*)(smem + L_W2 + (hw + 4 * i) * 1024 + lane * 16) = t2[i];
        }
      }
      const __amdgpu_buffer_rsrc_t ro = mkrs(wo + (size_t)h * DH * DM, NO_PREP ? 0 : DH * DM * 2);
      const int ooff = g4 * 8 * (DM * 2) + (hw < 3 ? c16 : (c16 & 7)) * 16;
      const bool sel = hw == 3 && c16 >= 8;
      v4u* dst = (v4u*)(smem + L_WOF + hw * 32768) + lane;
#pragma unroll
      for (int ks = 0; ks < 8; ++ks) {
        __builtin_amdgcn_s_sleep(PREP_SLEEP);
        PIN();
        v4u r[8];
#pragma unroll
        for (int jj = 0; jj < 8; ++jj) r[jj] = LD128(ro, ooff, (ks * 32 + jj) * (DM * 2) + hw * 256, AUX_WO);
        if (hw < 3) {
#pragma unroll
          for (int e = 4; e < 8; ++e) {
            v4u t;
#pragma unroll
            for (int d = 0; d < 4; ++d) t[d] = __builtin_amdgcn_perm(r[2 * d + 1][e >> 1], r[2 * d][e >> 1], (e & 1) ? 0x07060302u : 0x05040100u);
            dst[(ks * 4 + e - 4) * 64] = t;
          }
        }
#pragma unroll
        for (int jj = 0; jj < 8; ++jj) { r[jj][0] = sel ? r[jj][2] : r[jj][0]; r[jj][1] = sel ? r[jj][3] : r[jj][1]; }
#pragma unroll
        for (int e = 0; e < 4; ++e)
#pragma unroll
          for (int d = 0; d < 4; ++d)
            wf[ks * 4 + e][d] = __builtin_amdgcn_perm(r[2 * d + 1][e >> 1], r[2 * d][e >> 1], (e & 1) ? 0x07060302u : 0x05040100u);
        asm volatile("" : "+v"(wf[ks * 4]), "+v"(wf[ks * 4 + 1]), "+v"(wf[ks * 4 + 2]), "+v"(wf[ks * 4 + 3]) :: "memory");
      }
      STAMP(3);
      if (hw == 0) {
        SPIN(SPIN_CAP) {
          PIN();
          const v4u r0 = LD128(rws, lane * 16, WS_READY, AUX_SC1), r1 = LD128(rws, lane * 16, WS_READY + 1024, AUX_SC1);
          const bool ok = r0[0] == tagv[0] && r0[1] == tagv[1] && r0[2] == tagv[0] && r0[3] == tagv[1] && r1[0] == tagv[0] && r1[1] == tagv[1] && r1[2] == tagv[0] && r1[3] == tagv[1];
          if (__all(ok)) break;
          __builtin_amdgcn_s_sleep(16);
        }
        ST64(nextep, rws, (g == 0 && lane == 0) ? 0 : (int)OOB, WS_EPOCH, AUX_SC1);
        SPIN(LSPIN_CAP) { PIN(); if (misc[12] >= 4u) break; __builtin_amdgcn_s_sleep(16); }
        PIN();
        const float mo = u2f(misc[2 * (lane & 3)]);
        const v2u mlo = {nosent(mo), nosent(u2f(misc[2 * (lane & 3) + 1]))};
        ST64(mlo, rws, lane < 4 ? lane * 8 : (int)OOB, WS_ML + g * 32, AUX_SC1);
        v2u ml;
        SPIN(SPIN_CAP) {
          PIN();
          ml = LD64(rws, c16 * 8, WS_ML + (g >> 2) * 128, AUX_SC1);
          if (__all(ml[0] != SENT && ml[1] != SENT)) break;
          __builtin_amdgcn_s_sleep(16);
        }
        const float M = row_max(u2f(ml[0]));
        const float L = row_sum(u2f(ml[1]) * __builtin_amdgcn_exp2f((u2f(ml[0]) - M) * LOG2E));
        if (lane < 4) misc[8 + lane] = f2u(__builtin_amdgcn_exp2f((mo - M) * LOG2E) / L);
      }
      if (hw == 1) {
        SPIN(LSPIN_CAP) { PIN(); if (misc[16] != 0u) break; __builtin_amdgcn_s_sleep(32); }
        unsigned len;
        asm volatile("s_mov_b32 %0, .Lfd_code_end-.Lfd_code_begin" : "=s"(len));
        const __amdgpu_buffer_rsrc_t rc = mkrs((const void*)pc0, len);
        const int cbase = len > 16384u ? (int)(len - 16384u) : 0;
        v4u t[16];
#pragma unroll
        for (int i = 0; i < 16; ++i) t[i] = LD128(rc, lane * 16, cbase + i * 1024, 0);
        const unsigned dummy = __builtin_amdgcn_raw_buffer_load_b16(mkrs(out, NB * DM * 2), 0, 0, 0);
#pragma unroll
        for (int i = 0; i < 16; ++i) USE(t[i]);
        USE(dummy);
      }
      STAMP(4);
      BARRIER();
    }
    STAMP(5);
    const __amdgpu_buffer_rsrc_t rt = rws;
    volatile LAS unsigned* tm = misc;
    if (trole != 3) {
      const unsigned char* ol = smem + L_OL + c16 * OL_STRIDE + g4 * 16;
      const float wgt = u2f(misc[8 + g4]);
      f32x4 d[4];
#pragma unroll
      for (int e = 0; e < 4; ++e) d[e] = f32x4{0.f, 0.f, 0.f, 0.f};
      bf16x8 af[8];
#pragma unroll
      for (int ks = 0; ks < 8; ++ks) af[ks] = *(const bf16x8*)(ol + ks * 64);
      __builtin_amdgcn_sched_barrier(0);
#pragma unroll
      for (int ks = 0; ks < 8; ++ks) {
#pragma unroll
        for (int e = 0; e < 4; ++e) d[e] = __builtin_amdgcn_mfma_f32_16x16x32_bf16(af[ks], __builtin_bit_cast(bf16x8, wf[ks * 4 + e]), d[e], 0, 0, 0);
      }
      v4u y;
#pragma unroll
      for (int e = 0; e < 4; ++e) {
        float t = ((d[e][0] + d[e][1]) + (d[e][2] + d[e][3])) * wgt;
        t = swap16_add(t, t); t = swap32_add(t, t);
        y[e] = nosent(t);
      }
      {
        const int yo = trole == 7 ? (c16 & 7) * 2048 + (c16 >> 3) * 16 : c16 * 2048;
        ST128(y, rt, lane < 16 ? yo : (int)OOB, WS_Y + (unsigned)(b * NUNIT + (trole & 3) * 16) * 2048u + (g & 63) * 32 + (trole < 4 ? 16 : 0), AUX_SC1);
      }
      STAMP(6);
      if (trole >= 4 && is_m) {
        bf16x8 a1[14];
        s16x4 a2[7];
        {
          const int q = c16 >> 2, p = c16 & 3;
          const LAS unsigned char* s1 = (const LAS unsigned char*)(smem + L_W1) + (g4 * 8 + q) * 32 + p * 8;
          const LAS unsigned char* s2 = (const LAS unsigned char*)(smem + L_W2) + (g4 * 4 + q) * (DM * 2) + (hw * 112 + p * 4) * 2;
#pragma unroll
          for (int s = 0; s < 14; ++s) {
            const bf16x4 lo = __builtin_amdgcn_ds_read_tr16_b64_v4bf16((LAS bf16x4*)(s1 + s * 1024));
            const bf16x4 hi = __builtin_amdgcn_ds_read_tr16_b64_v4bf16((LAS bf16x4*)(s1 + s * 1024 + 128));
            a1[s] = __builtin_shufflevector(lo, hi, 0, 1, 2, 3, 4, 5, 6, 7);
          }
#pragma unroll
          for (int t = 0; t < 7; ++t) a2[t] = __builtin_bit_cast(s16x4, __builtin_amdgcn_ds_read_tr16_b64_v4bf16((LAS bf16x4*)(s2 + t * 32)));
        }
        SPIN(LSPIN_CAP) { PIN(); if (tm[14] != 0u) break; __builtin_amdgcn_s_sleep(2); }
        PIN();
        v4u h0, h1;
        {
          const unsigned hb = WS_HB + (unsigned)(mi % HREP) * (NB * DM * 4) + hw * (DM * 4);
          const int vo = lane < 56 ? lane * 32 : (int)OOB;
          SPIN(SPIN_CAP) {
            PIN();
            h0 = LD128(rt, vo, hb, AUX_SC1); h1 = LD128(rt, vo, hb + 16, AUX_SC1);
            const bool ok = valid4(h0) & valid4(h1);
            if (__all(ok)) break;
          }
        }
        STAMP(7);
        {
          float v[8];
#pragma unroll
          for (int k = 0; k < 4; ++k) { v[k] = u2f(h0[k]); v[k + 4] = u2f(h1[k]); }
          ln8(v, lane);
          const v4u hn = {pk2(v[0], v[1]), pk2(v[2], v[3]), pk2(v[4], v[5]), pk2(v[6], v[7])};
          if (lane < 56) *(v4u*)(smem + L_HN + hw * HN_STRIDE + lane * 16) = hn;
          LGKM0();
          if (lane == 0) LDS_INC(13);
          SPIN(LSPIN_CAP) { PIN(); if (tm[13] >= 4u) break; }
          PIN();
        }
        f32x4 d1a = {0.f, 0.f, 0.f, 0.f}, d1b = {0.f, 0.f, 0.f, 0.f};
        {
          const unsigned char* hp = smem + L_HN + (c16 & 3) * HN_STRIDE + g4 * 16;
          bf16x8 bfr[14];
#pragma unroll
          for (int s = 0; s < 14; ++s) bfr[s] = *(const bf16x8*)(hp + s * 64);
          __builtin_amdgcn_sched_barrier(0);
#pragma unroll
          for (int s = 0; s < 14; s += 2) {
            d1a = __builtin_amdgcn_mfma_f32_16x16x32_bf16(a1[s], bfr[s], d1a, 0, 0, 0);
            d1b = __builtin_amdgcn_mfma_f32_16x16x32_bf16(a1[s + 1], bfr[s + 1], d1b, 0, 0, 0);
          }
        }
        float sv[4];
#pragma unroll
        for (int r = 0; r < 4; ++r) { const float t = rbf(d1a[r] + d1b[r]); sv[r] = t * __builtin_amdgcn_rcpf(1.0f + __builtin_amdgcn_exp2f(-t * LOG2E)); }
        const v2u hbp = {pk2(sv[0], sv[1]), pk2(sv[2], sv[3])};
        const s16x4 b2 = __builtin_bit_cast(s16x4, hbp);
        const int vo = c16 < 4 ? c16 * (NUNIT * NM * 32) + (g4 >> 1) * (NM * 32) + (g4 & 1) * 16 : (int)OOB;
        f32x4 o2[7];
#pragma unroll
        for (int t = 0; t < 7; ++t) o2[t] = __builtin_amdgcn_mfma_f32_16x16x16bf16_1k(a2[t], b2, f32x4{0.f, 0.f, 0.f, 0.f}, 0, 0, 0);
        __builtin_amdgcn_sched_barrier(0);
#pragma unroll
        for (int t = 0; t < 7; ++t) {
          const f32x4 o = o2[t];
          const v4u ov = {nosent(o[0]), nosent(o[1]), nosent(o[2]), nosent(o[3])};
          ST128(ov, rt, vo, WS_O + (unsigned)(hw * 14 + 2 * t) * (NM * 32u) + mi * 32, AUX_SC1);
        }
        STAMP(2);
      }
    } else if (unit < NUNIT) {
      const int col = unit * 8 + 4 * (lane & 1) + 2 * half + (g4 & 1);
      const float xv = u2f((unsigned)__builtin_amdgcn_raw_buffer_load_b16(rx, col * 2, b * (DM * 2), 0) << 16);
      const __amdgpu_buffer_rsrc_t rout = mkrs(out, NB * DM * 2);
      const unsigned yb = WS_Y + (unsigned)(b * NUNIT + unit) * 2048u, ob = WS_O + (unsigned)(b * NUNIT + unit) * (NM * 32u);
      v4u p0 = {SENT, SENT, SENT, SENT}, p1 = p0;
      SPIN(SPIN_CAP) {
        PIN();
        const bool k0 = valid4(p0), k1 = valid4(p1);
        if (__all(k0 & k1)) break;
        const v4u n0 = LD128(rt, k0 ? (int)OOB : lane * 16, yb, AUX_SC1), n1 = LD128(rt, k1 ? (int)OOB : lane * 16, yb + 1024, AUX_SC1);
        p0 = k0 ? p0 : n0; p1 = k1 ? p1 : n1;
      }
      STAMP(6);
      const float hv = rbf(xv + rbf(reduce_pieces(asf(p0) + asf(p1))));
      const int rep = (lane >> 1) & 7;
      ST32(nosent(hv), rt, rep < HREP ? rep * (NB * DM * 4) + col * 4 : (int)OOB, WS_HB + b * (DM * 4), AUX_SC1);
      if (lane == 0) tm[14] = 1u;
      v4u p2 = {SENT, SENT, SENT, SENT}, p3 = lane < 32 ? p2 : v4u{0u, 0u, 0u, 0u};
      p0 = p2; p1 = p2;
      __builtin_amdgcn_s_sleep(F_SLEEP);
      SPIN(SPIN_CAP) {
        PIN();
        const bool k0 = valid4(p0), k1 = valid4(p1), k2 = valid4(p2), k3 = valid4(p3);
        if (__all(k0 & k1 & k2 & k3)) break;
        const v4u n0 = LD128(rt, k0 ? (int)OOB : lane * 16, ob, AUX_SC1), n1 = LD128(rt, k1 ? (int)OOB : lane * 16, ob + 1024, AUX_SC1);
        const v4u n2 = LD128(rt, k2 ? (int)OOB : lane * 16, ob + 2048, AUX_SC1), n3 = LD128(rt, k3 ? (int)OOB : lane * 16, ob + 3072, AUX_SC1);
        p0 = k0 ? p0 : n0; p1 = k1 ? p1 : n1; p2 = k2 ? p2 : n2; p3 = k3 ? p3 : n3;
      }
      STAMP(7);
      const float ov = hv + rbf(reduce_pieces((asf(p0) + asf(p1)) + (asf(p2) + asf(p3))));
      __builtin_amdgcn_raw_buffer_store_b16(__builtin_bit_cast(u16, (__bf16)ov), rout, rep == 0 ? col * 2 : (int)OOB, b * (DM * 2), 0);
      STAMP(1);
      asm volatile(".Lfd_code_end:");
    }
  }
}

extern "C" void kernel_launch(void* const* d_in, const int* in_sizes, int n_in, void* d_out, int out_size, void* d_ws, size_t ws_size, hipStream_t stream) {
  fused_decode<<<NWG, NTHR, 0, stream>>>((const u16*)d_in[0], (const u16*)d_in[1], (const u16*)d_in[2], (const u16*)d_in[3], (const u16*)d_in[4],
                                         (const u16*)d_in[6], (const u16*)d_in[7], (u16*)d_out, (unsigned char*)d_ws);
}
```

```cpp
#include <hip/hip_runtime.h>
#include <stdint.h>

typedef unsigned v4u __attribute__((ext_vector_type(4)));
typedef unsigned v2u __attribute__((ext_vector_type(2)));
typedef float f32x4 __attribute__((ext_vector_type(4)));
typedef float f32x2 __attribute__((ext_vector_type(2)));
typedef __bf16 bf16x2 __attribute__((ext_vector_type(2)));
typedef __bf16 bf16x4 __attribute__((ext_vector_type(4)));
typedef __bf16 bf16x8 __attribute__((ext_vector_type(8)));
typedef short s16x4 __attribute__((ext_vector_type(4)));
typedef unsigned short u16;
typedef unsigned long long u64;
#define LAS __attribute__((address_space(3)))

constexpr int NB = 4, DM = 448, NH = 16, SK = 8192, DH = 256, FF = 1792;
constexpr int NWG = 256, NTHR = 512;
constexpr int NL = 256;
#define WINDOW 8
constexpr int WIN = WINDOW;
constexpr int NUNIT = 56;
constexpr int NM = 112;
#define HREP 4
constexpr unsigned SENT = 0xFFFFFFFFu;
constexpr int AUX_NT = 2, AUX_SC1 = 16;
#define AUX_WQ 2
#define AUX_WO 2
#define MAPPING 1
#define NO_PREP 0
#define NO_QWAIT 0
#define VDEF_FROM 256
#define VDEF_TO 256
#define CODE_PF 1
#define PF_AT 13
#define PREP_SLEEP 100
#define F_SLEEP 24
constexpr unsigned OOB = 0x7FFFFFF0u;
constexpr float LOG2E = 1.4426950408889634f;

constexpr unsigned WS_EPOCH = 0, WS_READY = 4096, WS_ML = 8192, WS_HB = 16384, WS_Q = 1u << 17, WS_Y = 1u << 18, WS_O = 1u << 20, WS_BYTES = 2u << 20;
constexpr unsigned WS_STAMP = 64u << 20;
constexpr int L_WOF = 0;
constexpr int L_W1 = L_WOF + 3 * 32 * 1024;
constexpr int L_W2 = L_W1 + 14336;
constexpr int L_SC = L_W2 + 14336;
constexpr int L_QB = L_SC + 8192;
constexpr int L_OL = L_QB + 512;
constexpr int OL_STRIDE = 528;
constexpr int L_HN = L_OL + 16 * OL_STRIDE;
constexpr int HN_STRIDE = 912;
constexpr int L_MISC = L_HN + 4 * HN_STRIDE;
constexpr int L_BYTES = L_MISC + 128;
static_assert(L_BYTES <= 163840 && L_MISC % 16 == 0 && L_HN % 16 == 0, "lds");

__device__ __forceinline__ float u2f(unsigned u) { return __builtin_bit_cast(float, u); }
__device__ __forceinline__ unsigned f2u(float f) { return __builtin_bit_cast(unsigned, f); }
__device__ __forceinline__ float rbf(float f) { return (float)(__bf16)f; }
__device__ __forceinline__ unsigned pk2(float lo, float hi) { bf16x2 v = {(__bf16)lo, (__bf16)hi}; return __builtin_bit_cast(unsigned, v); }
__device__ __forceinline__ float blo(unsigned u) { return u2f(u << 16); }
__device__ __forceinline__ float bhi(unsigned u) { return u2f(u & 0xffff0000u); }
__device__ __forceinline__ float dot2(unsigned a, unsigned b, float c) {
  return __builtin_amdgcn_fdot2_f32_bf16(__builtin_bit_cast(bf16x2, a), __builtin_bit_cast(bf16x2, b), c, false);
}
template <int CTRL> __device__ __forceinline__ float dpp(float x) {
  return __builtin_bit_cast(float, __builtin_amdgcn_mov_dpp(__builtin_bit_cast(int, x), CTRL, 0xf, 0xf, true));
}
constexpr int XOR1 = 0xB1, XOR2 = 0x4E, XOR7 = 0x141, ROR8 = 0x128, ROR4 = 0x124;
__device__ __forceinline__ float swap16_add(float a, float b) {
  auto s = __builtin_amdgcn_permlane16_swap(f2u(a), f2u(b), false, false);
  const unsigned s0 = s[0], s1 = s[1];
  return u2f(s0) + u2f(s1);
}
__device__ __forceinline__ float swap32_add(float a, float b) {
  auto s = __builtin_amdgcn_permlane32_swap(f2u(a), f2u(b), false, false);
  const unsigned s0 = s[0], s1 = s[1];
  return u2f(s0) + u2f(s1);
}
__device__ __forceinline__ float swap16_max(float a) {
  auto s = __builtin_amdgcn_permlane16_swap(f2u(a), f2u(a), false, false);
  const unsigned s0 = s[0], s1 = s[1];
  return fmaxf(u2f(s0), u2f(s1));
}
__device__ __forceinline__ float swap32_max(float a) {
  auto s = __builtin_amdgcn_permlane32_swap(f2u(a), f2u(a), false, false);
  const unsigned s0 = s[0], s1 = s[1];
  return fmaxf(u2f(s0), u2f(s1));
}
__device__ __forceinline__ float row_sum(float x) { x += dpp<ROR8>(x); x += dpp<ROR4>(x); x += dpp<XOR2>(x); x += dpp<XOR1>(x); return x; }
__device__ __forceinline__ float row_max(float x) { x = fmaxf(x, dpp<ROR8>(x)); x = fmaxf(x, dpp<ROR4>(x)); x = fmaxf(x, dpp<XOR2>(x)); x = fmaxf(x, dpp<XOR1>(x)); return x; }
__device__ __forceinline__ float wave_sum(float x) { x = row_sum(x); x = swap16_add(x, x); return swap32_add(x, x); }
__device__ __forceinline__ float wave_max(float x) { x = row_max(x); x = swap16_max(x); return swap32_max(x); }
__device__ __forceinline__ unsigned nosent(float f) { const unsigned u = f2u(f); return u < 0xFFFFFFFEu ? u : 0xFFFFFFFEu; }

__device__ __forceinline__ __amdgpu_buffer_rsrc_t mkrs(const void* p, unsigned bytes) { return __builtin_amdgcn_make_buffer_rsrc((void*)p, 0, bytes, 0x00020000); }
#define LD128(rs, vo, so, aux) __builtin_amdgcn_raw_buffer_load_b128(rs, vo, so, aux)
#define LD64(rs, vo, so, aux) __builtin_amdgcn_raw_buffer_load_b64(rs, vo, so, aux)
#define ST128(v, rs, vo, so, aux) __builtin_amdgcn_raw_buffer_store_b128(v, rs, vo, so, aux)
#define ST64(v, rs, vo, so, aux) __builtin_amdgcn_raw_buffer_store_b64(v, rs, vo, so, aux)
#define ST32(v, rs, vo, so, aux) __builtin_amdgcn_raw_buffer_store_b32(v, rs, vo, so, aux)
#define PIN() asm volatile("" ::: "memory")
#define USE(x) asm volatile("" :: "v"(x) : "memory")
#define REDEF(x) asm volatile("" : "+v"(x) :: "memory")
#define BARRIER() asm volatile("s_waitcnt lgkmcnt(0)\n\ts_barrier" ::: "memory")
#define DRAIN() asm volatile("s_waitcnt vmcnt(0)" ::: "memory")
#define LGKM0() asm volatile("s_waitcnt lgkmcnt(0)" ::: "memory")
constexpr int SPIN_CAP = 1 << 16;
constexpr int LSPIN_CAP = 1 << 20;

#define STAMP(i) do {} while (0)

__device__ __forceinline__ void ln8(float (&v)[8], int lane) {
  float s = ((v[0] + v[1]) + (v[2] + v[3])) + ((v[4] + v[5]) + (v[6] + v[7]));
  const float mean = wave_sum(s) * (1.0f / DM);
  float q = 0.f;
#pragma unroll
  for (int e = 0; e < 8; ++e) { const float d = v[e] - mean; q = fmaf(d, d, q); }
  q = lane < 56 ? q : 0.f;
  const float var = wave_sum(q) * (1.0f / DM);
  const float mb = rbf(mean);
  const float den = rbf(__builtin_amdgcn_sqrtf(rbf(rbf(var) + 1e-5f)));
  const float inv = __builtin_amdgcn_rcpf(den);
#pragma unroll
  for (int e = 0; e < 8; ++e) v[e] = rbf(v[e] - mb) * inv;
}
__device__ __forceinline__ float reduce_pieces(f32x4 v) {
  const float a = swap32_add(v[0], v[2]), b = swap32_add(v[1], v[3]);
  float c = swap16_add(a, b);
  c += dpp<ROR8>(c); c += dpp<ROR4>(c); c += dpp<XOR2>(c);
  return c;
}
__device__ __forceinline__ bool valid4(v4u a) { const unsigned m0 = a[0] > a[1] ? a[0] : a[1], m1 = a[2] > a[3] ? a[2] : a[3]; return (m0 > m1 ? m0 : m1) != SENT; }
__device__ __forceinline__ f32x4 asf(v4u a) { return __builtin_bit_cast(f32x4, a); }

#define SPIN(cap) _Pragma("unroll 1") for (int spin = 0; spin < (cap); ++spin)
#define LDS_INC(w) __hip_atomic_fetch_add((LAS unsigned*)(smem + L_MISC) + (w), 1u, __ATOMIC_RELAXED, __HIP_MEMORY_SCOPE_WORKGROUP)

__global__ void __launch_bounds__(NTHR, 2)
fused_decode(const u16* x, const u16* Kc, const u16* Vc, const u16* wq, const u16* wo, const u16* w1, const u16* w2, u16* out, unsigned char* ws) {
  __shared__ __attribute__((aligned(16))) unsigned char smem[L_BYTES];
  const int tid = threadIdx.x, lane = tid & 63;
  int wave = __builtin_amdgcn_readfirstlane(tid >> 6);
  const int g = ((blockIdx.x >> 6) << 6) | (((blockIdx.x & 7) + 8 * ((blockIdx.x >> 3) & 1)) << 2) | ((blockIdx.x >> 4) & 3);
  const int b = g >> 6, h = (g >> 2) & 15;
  const int half = lane >> 5, c32 = lane & 31, g4 = lane >> 4, c16 = lane & 15;
  volatile LAS unsigned* misc = (volatile LAS unsigned*)(smem + L_MISC);
  const __amdgpu_buffer_rsrc_t rws = mkrs(ws, WS_BYTES);
  const __amdgpu_buffer_rsrc_t rk = mkrs(Kc + (size_t)g * (2048 * DH), 2048 * DH * 2);
  const __amdgpu_buffer_rsrc_t rx = mkrs(x, NB * DM * 2);
  const int voff = tid * 16;
  const int hw = wave - 4;
  v4u kb[32];
  u64 pc0;
  asm volatile("s_getpc_b64 %0\n.Lfd_code_begin:" : "=s"(pc0));
  STAMP(0);
  if (wave < 4) {
#pragma unroll
    for (int i = 0; i < WIN; ++i) kb[i] = LD128(rk, voff, i * 4096, AUX_NT);
  } else {
    const int j = b * 4 + (g & 3);
    const __amdgpu_buffer_rsrc_t rq = mkrs(wq + h * DH + j * 16, (DM - 1) * (NH * DH * 2) + 32);
    kb[0] = LD128(rx, lane < 56 ? lane * 16 : (int)OOB, hw * (DM * 2), 0);
#pragma unroll
    for (int i = 0; i < 4; ++i) kb[1 + i] = LD128(rq, (lane >> 1) * (NH * DH * 2) + (lane & 1) * 16, (hw + 4 * i) * 32 * (NH * DH * 2), AUX_WQ);
    kb[5] = LD128(rws, 0, WS_EPOCH, AUX_SC1);
#pragma unroll
    for (int i = 6; i < WIN; ++i) asm volatile("" : "=v"(kb[i]));
    if (tid == 256) { misc[12] = 0u; misc[13] = 0u; misc[14] = 0u; misc[15] = 0u; misc[16] = 0u; }
  }
  BARRIER();
  asm volatile("" : "+s"(wave));

  const int unit = g & 63;
  const bool is_m = (g & 15) < 7;
  const int mi = (g >> 4) * 7 + (g & 15);
  v4u wf[32];
  {
    const int trole = wave;
    if (wave < 4) {
      const __amdgpu_buffer_rsrc_t rv = mkrs(Vc + (size_t)g * (2048 * DH), 2048 * DH * 2);
#pragma unroll
      for (int i = WIN; i < 32; ++i) { USE(kb[i - WIN]); kb[i] = LD128(rk, voff, i * 4096, AUX_NT); PIN(); }
      STAMP(1);
      BARRIER();
      STAMP(2);
      const v4u qv = *(const v4u*)(smem + L_QB + c32 * 16);
      float* sc = (float*)(smem + L_SC) + (wave * 2 + half) * 256;
      float m = -INFINITY;
#define DOT(i) do { float d_ = dot2(kb[i][0], qv[0], 0.f); d_ = dot2(kb[i][1], qv[1], d_); d_ = dot2(kb[i][2], qv[2], d_); d_ = dot2(kb[i][3], qv[3], d_); \
                    asm volatile("" : "+v"(d_)); xs[i] = d_; } while (0)
#define TR_STEP(HALF, CTRL, BIT) _Pragma("unroll") for (int hh = 0; hh < (HALF); ++hh) { \
      const float lo_ = xs[hh], hi_ = xs[hh + (HALF)]; const float t1_ = lo_ + dpp<CTRL>(lo_), t2_ = hi_ + dpp<CTRL>(hi_); \
      xs[hh] = (c32 & (BIT)) ? t2_ : t1_; }
#define REDUCE(grp) do { \
      _Pragma("unroll") for (int i = 0; i < 16; ++i) xs[i] = swap16_add(xs[i], xs[i + 16]); \
      TR_STEP(8, ROR8, 8) TR_STEP(4, XOR7, 4) TR_STEP(2, XOR2, 2) TR_STEP(1, XOR1, 1) \
      const float s_ = rbf(xs[0]) * 0.0625f; m = fmaxf(m, s_); sc[(grp) * 32 + c32] = s_; } while (0)
      {
        float xs[32];
#pragma unroll
        for (int i = 0; i < 32; ++i) {
          REDEF(kb[i]);
          DOT(i);
          if (i < WIN) { PIN(); kb[i] = LD128(rk, voff, (32 + i) * 4096, AUX_NT); PIN(); }
        }
        REDUCE(0);
      }
#pragma unroll 1
      for (int gi = 1; gi < 8; ++gi) {
        float xs[32];
        const __amdgpu_buffer_rsrc_t rn = gi < 7 ? rk : rv;
        const int offn = gi < 7 ? (gi + 1) * 32 * 4096 : 0;
#pragma unroll
        for (int i = 0; i < 32; ++i) {
          USE(kb[i]);
          if (i + WIN < 32) kb[i + WIN] = LD128(rk, voff, (gi * 32 + i + WIN) * 4096, AUX_NT);
          else kb[i + WIN - 32] = LD128(rn, voff, offn + (i + WIN - 32) * 4096, AUX_NT);
          REDEF(kb[i]);
          DOT(i);
        }
        REDUCE(gi);
      }
      STAMP(3);
      m = wave_max(m);
      {
        LGKM0();
        f32x4* ps = (f32x4*)(smem + L_SC) + wave * 128 + lane * 2;
        f32x4 a = ps[0], bq = ps[1];
        const float mm = m * LOG2E;
#pragma unroll
        for (int e = 0; e < 4; ++e) { a[e] = __builtin_amdgcn_exp2f(fmaf(a[e], LOG2E, -mm)); bq[e] = __builtin_amdgcn_exp2f(fmaf(bq[e], LOG2E, -mm)); }
        ps[0] = a; ps[1] = bq;
        float l = ((a[0] + a[1]) + (a[2] + a[3])) + ((bq[0] + bq[1]) + (bq[2] + bq[3]));
        l = wave_sum(l);
        if (lane == 0) { misc[2 * wave] = f2u(m); misc[2 * wave + 1] = f2u(l); }
        LGKM0();
        if (lane == 0) LDS_INC(12);
      }
      float acc[8];
#pragma unroll
      for (int k = 0; k < 8; ++k) acc[k] = 0.f;
#define VLOOP(AUXV) \
_Pragma("unroll 1") \
      for (int gi = 0; gi < NL / 16; ++gi) { \
        f32x4 p4[4]; \
_Pragma("unroll") \
        for (int i = 0; i < 4; ++i) p4[i] = *(const f32x4*)(sc + gi * 16 + i * 4); \
        if (gi == PF_AT && tid == 0) misc[16] = 1u; \
_Pragma("unroll") \
        for (int i = 0; i < 16; ++i) { \
          USE(kb[i]); \
          kb[(i + WIN) & 15] = LD128(rv, voff, (gi * 16 + i + WIN) * 4096, AUXV); \
          REDEF(kb[i]); \
          const float p = p4[i >> 2][i & 3]; \
_Pragma("unroll") \
          for (int k = 0; k < 4; ++k) { acc[2 * k] = fmaf(p, blo(kb[i][k]), acc[2 * k]); acc[2 * k + 1] = fmaf(p, bhi(kb[i][k]), acc[2 * k + 1]); } \
          asm volatile("" : "+v"(acc[0]), "+v"(acc[1]), "+v"(acc[2]), "+v"(acc[3]), "+v"(acc[4]), "+v"(acc[5]), "+v"(acc[6]), "+v"(acc[7])); \
        } \
      } \

      if (g >= VDEF_FROM && g < VDEF_TO) { VLOOP(0) } else { VLOOP(AUX_NT) }
      STAMP(4);
      {
        v4u hi, lo;
#pragma unroll
        for (int k = 0; k < 4; ++k) {
          hi[k] = pk2(acc[2 * k], acc[2 * k + 1]);
          lo[k] = pk2(acc[2 * k] - blo(hi[k]), acc[2 * k + 1] - bhi(hi[k]));
        }
        unsigned char* ol = smem + L_OL + (wave * 4 + half * 2) * OL_STRIDE + c32 * 16;
        *(v4u*)ol = hi; *(v4u*)(ol + OL_STRIDE) = lo;
      }
      BARRIER();
      if (wave < 3) {
        const v4u* src = (const v4u*)(smem + L_WOF + wave * 32768) + lane;
#pragma unroll
        for (int i = 0; i < 32; ++i) wf[i] = src[i * 64];
      }
    } else {
      {
        float v[8];
#pragma unroll
        for (int k = 0; k < 4; ++k) { v[2 * k] = blo(kb[0][k]); v[2 * k + 1] = bhi(kb[0][k]); }
        ln8(v, lane);
        const v4u xn = {pk2(v[0], v[1]), pk2(v[2], v[3]), pk2(v[4], v[5]), pk2(v[6], v[7])};
        if (lane < 56) *(v4u*)(smem + L_HN + hw * HN_STRIDE + lane * 16) = xn;
#pragma unroll
        for (int i = 0; i < 4; ++i) if (hw + 4 * i < 14) *(v4u*)(smem + L_W1 + (hw + 4 * i) * 1024 + lane * 16) = kb[1 + i];
        LGKM0();
        if (lane == 0) LDS_INC(15);
      }
      const unsigned e0 = __builtin_amdgcn_readfirstlane(kb[5][0]), e1 = __builtin_amdgcn_readfirstlane(kb[5][1]);
      v2u tagv, nextep;
      unsigned qtag = e0 + 0x9E3779B9u;
      qtag ^= qtag >> 16; qtag *= 0x85EBCA6Bu; qtag ^= qtag >> 13; qtag *= 0xC2B2AE35u; qtag ^= qtag >> 16;
      {
        u64 tag = ((u64)e1 << 32) | e0;
        const u64 ne = tag + 1;
        tag += 0x9E3779B97F4A7C15ull; tag = (tag ^ (tag >> 30)) * 0xBF58476D1CE4E5B9ull; tag = (tag ^ (tag >> 27)) * 0x94D049BB133111EBull; tag ^= tag >> 31;
        tagv = v2u{(unsigned)tag, (unsigned)(tag >> 32)};
        nextep = v2u{(unsigned)ne, (unsigned)(ne >> 32)};
      }
      if (hw == 0) {
        SPIN(LSPIN_CAP) { PIN(); if (misc[15] >= 4u) break; }
        PIN();
        const int q = c16 >> 2, p = c16 & 3;
        const LAS unsigned char* s1 = (const LAS unsigned char*)(smem + L_W1) + (g4 * 8 + q) * 32 + p * 8;
        const unsigned char* hp = smem + L_HN + (c16 & 3) * HN_STRIDE + g4 * 16;
        bf16x8 a1[14], bfr[14];
#pragma unroll
        for (int s = 0; s < 14; ++s) {
          const bf16x4 lo = __builtin_amdgcn_ds_read_tr16_b64_v4bf16((LAS bf16x4*)(s1 + s * 1024));
          const bf16x4 hi = __builtin_amdgcn_ds_read_tr16_b64_v4bf16((LAS bf16x4*)(s1 + s * 1024 + 128));
          a1[s] = __builtin_shufflevector(lo, hi, 0, 1, 2, 3, 4, 5, 6, 7);
          bfr[s] = *(const bf16x8*)(hp + s * 64);
        }
        __builtin_amdgcn_sched_barrier(0);
        f32x4 da = {0.f, 0.f, 0.f, 0.f}, db = {0.f, 0.f, 0.f, 0.f};
#pragma unroll
        for (int s = 0; s < 14; s += 2) {
          da = __builtin_amdgcn_mfma_f32_16x16x32_bf16(a1[s], bfr[s], da, 0, 0, 0);
          db = __builtin_amdgcn_mfma_f32_16x16x32_bf16(a1[s + 1], bfr[s + 1], db, 0, 0, 0);
        }
        da += db;
        const v4u qg = {pk2(da[0], da[1]), qtag, pk2(da[2], da[3]), qtag};
        ST128(qg, rws, c16 < 4 ? c16 * 1024 + g4 * 16 : (int)OOB, WS_Q + h * 4096 + (b * 4 + (g & 3)) * 64, AUX_SC1);
        STAMP(1);
      } else if (hw == 1) {
        v4u qg;
        SPIN(SPIN_CAP) {
          PIN();
          qg = LD128(rws, lane * 16, WS_Q + h * 4096 + b * 1024, AUX_SC1);
          if (NO_QWAIT || __all(qg[1] == qtag && qg[3] == qtag)) break;
        }
        *(v2u*)(smem + L_QB + lane * 8) = v2u{qg[0], qg[2]};
        STAMP(1);
      }
      BARRIER();
      if (hw == 0) {
        const v4u s4 = {SENT, SENT, SENT, SENT};
        if (unit < NUNIT) {
          const unsigned yb = WS_Y + (unsigned)(b * NUNIT + unit) * 2048u, ob = WS_O + (unsigned)(b * NUNIT + unit) * (NM * 32u);
          ST128(s4, rws, lane * 16, yb, AUX_SC1); ST128(s4, rws, lane * 16, yb + 1024, AUX_SC1);
          ST128(s4, rws, lane * 16, ob, AUX_SC1); ST128(s4, rws, lane * 16, ob + 1024, AUX_SC1); ST128(s4, rws, lane * 16, ob + 2048, AUX_SC1);
          ST128(s4, rws, lane < 32 ? lane * 16 : (int)OOB, ob + 3072, AUX_SC1);
          ST128(s4, rws, lane < 2 * HREP ? (lane >> 1) * (NB * DM * 4) + (lane & 1) * 16 : (int)OOB, WS_HB + (b * DM + unit * 8) * 4, AUX_SC1);
        }
        ST128(s4, rws, lane < 2 ? lane * 16 : (int)OOB, WS_ML + g * 32, AUX_SC1);
        DRAIN();
        ST64(tagv, rws, lane == 0 ? 0 : (int)OOB, WS_READY + g * 8, AUX_SC1);
      }
      if (is_m && !NO_PREP) {
        const __amdgpu_buffer_rsrc_t r1 = mkrs(w1 + mi * 16, (DM - 1) * (FF * 2) + 32);
        const __amdgpu_buffer_rsrc_t r2 = mkrs(w2 + (size_t)mi * 16 * DM, 16 * DM * 2);
        v4u t1[4], t2[4];
#pragma unroll
        for (int i = 0; i < 4; ++i) {
          t1[i] = LD128(r1, (lane >> 1) * (FF * 2) + (lane & 1) * 16, (hw + 4 * i) * 32 * (FF * 2), AUX_NT);
          t2[i] = LD128(r2, lane * 16, (hw + 4 * i) * 1024, AUX_NT);
        }
#pragma unroll
        for (int i = 0; i < 4; ++i) if (hw + 4 * i < 14) {
          *(v4u*)(smem + L_W1 + (hw + 4 * i) * 1024 + lane * 16) = t1[i];
          *(v4u*)(smem + L_W2 + (hw + 4 * i) * 1024 + lane * 16) = t2[i];
        }
      }
      const __amdgpu_buffer_rsrc_t ro = mkrs(wo + (size_t)h * DH * DM, NO_PREP ? 0 : DH * DM * 2);
      const int ooff = g4 * 8 * (DM * 2) + (hw < 3 ? c16 : (c16 & 7)) * 16;
      const bool sel = hw == 3 && c16 >= 8;
      v4u* dst = (v4u*)(smem + L_WOF + hw * 32768) + lane;
#pragma unroll
      for (int ks = 0; ks < 8; ++ks) {
        __builtin_amdgcn_s_sleep(PREP_SLEEP);
        PIN();
        v4u r[8];
#pragma unroll
        for (int jj = 0; jj < 8; ++jj) r[jj] = LD128(ro, ooff, (ks * 32 + jj) * (DM * 2) + hw * 256, AUX_WO);
        if (hw < 3) {
#pragma unroll
          for (int e = 4; e < 8; ++e) {
            v4u t;
#pragma unroll
            for (int d = 0; d < 4; ++d) t[d] = __builtin_amdgcn_perm(r[2 * d + 1][e >> 1], r[2 * d][e >> 1], (e & 1) ? 0x07060302u : 0x05040100u);
            dst[(ks * 4 + e - 4) * 64] = t;
          }
        }
#pragma unroll
        for (int jj = 0; jj < 8; ++jj) { r[jj][0] = sel ? r[jj][2] : r[jj][0]; r[jj][1] = sel ? r[jj][3] : r[jj][1]; }
#pragma unroll
        for (int e = 0; e < 4; ++e)
#pragma unroll
          for (int d = 0; d < 4; ++d)
            wf[ks * 4 + e][d] = __builtin_amdgcn_perm(r[2 * d + 1][e >> 1], r[2 * d][e >> 1], (e & 1) ? 0x07060302u : 0x05040100u);
        asm volatile("" : "+v"(wf[ks * 4]), "+v"(wf[ks * 4 + 1]), "+v"(wf[ks * 4 + 2]), "+v"(wf[ks * 4 + 3]) :: "memory");
      }
      STAMP(3);
      if (hw == 0) {
        SPIN(SPIN_CAP) {
          PIN();
          const v4u r0 = LD128(rws, lane * 16, WS_READY, AUX_SC1), r1 = LD128(rws, lane * 16, WS_READY + 1024, AUX_SC1);
          const bool ok = r0[0] == tagv[0] && r0[1] == tagv[1] && r0[2] == tagv[0] && r0[3] == tagv[1] && r1[0] == tagv[0] && r1[1] == tagv[1] && r1[2] == tagv[0] && r1[3] == tagv[1];
          if (__all(ok)) break;
          __builtin_amdgcn_s_sleep(16);
        }
        ST64(nextep, rws, (g == 0 && lane == 0) ? 0 : (int)OOB, WS_EPOCH, AUX_SC1);
        SPIN(LSPIN_CAP) { PIN(); if (misc[12] >= 4u) break; __builtin_amdgcn_s_sleep(16); }
        PIN();
        const float mo = u2f(misc[2 * (lane & 3)]);
        const v2u mlo = {nosent(mo), nosent(u2f(misc[2 * (lane & 3) + 1]))};
        ST64(mlo, rws, lane < 4 ? lane * 8 : (int)OOB, WS_ML + g * 32, AUX_SC1);
        v2u ml;
        SPIN(SPIN_CAP) {
          PIN();
          ml = LD64(rws, c16 * 8, WS_ML + (g >> 2) * 128, AUX_SC1);
          if (__all(ml[0] != SENT && ml[1] != SENT)) break;
          __builtin_amdgcn_s_sleep(16);
        }
        const float M = row_max(u2f(ml[0]));
        const float L = row_sum(u2f(ml[1]) * __builtin_amdgcn_exp2f((u2f(ml[0]) - M) * LOG2E));
        if (lane < 4) misc[8 + lane] = f2u(__builtin_amdgcn_exp2f((mo - M) * LOG2E) / L);
      }
      if (hw == 1) {
        SPIN(LSPIN_CAP) { PIN(); if (misc[16] != 0u) break; __builtin_amdgcn_s_sleep(32); }
        unsigned len;
        asm volatile("s_mov_b32 %0, .Lfd_code_end-.Lfd_code_begin" : "=s"(len));
        const __amdgpu_buffer_rsrc_t rc = mkrs((const void*)pc0, len);
        const int cbase = len > 16384u ? (int)(len - 16384u) : 0;
        v4u t[16];
#pragma unroll
        for (int i = 0; i < 16; ++i) t[i] = LD128(rc, lane * 16, cbase + i * 1024, 0);
        const unsigned dummy = __builtin_amdgcn_raw_buffer_load_b16(mkrs(out, NB * DM * 2), 0, 0, 0);
#pragma unroll
        for (int i = 0; i < 16; ++i) USE(t[i]);
        USE(dummy);
      }
      STAMP(4);
      BARRIER();
    }
    STAMP(5);
    const __amdgpu_buffer_rsrc_t rt = rws;
    volatile LAS unsigned* tm = misc;
    if (trole != 3) {
      const unsigned char* ol = smem + L_OL + c16 * OL_STRIDE + g4 * 16;
      const float wgt = u2f(misc[8 + g4]);
      f32x4 d[4];
#pragma unroll
      for (int e = 0; e < 4; ++e) d[e] = f32x4{0.f, 0.f, 0.f, 0.f};
      bf16x8 af[8];
#pragma unroll
      for (int ks = 0; ks < 8; ++ks) af[ks] = *(const bf16x8*)(ol + ks * 64);
      __builtin_amdgcn_sched_barrier(0);
#pragma unroll
      for (int ks = 0; ks < 8; ++ks) {
#pragma unroll
        for (int e = 0; e < 4; ++e) d[e] = __builtin_amdgcn_mfma_f32_16x16x32_bf16(af[ks], __builtin_bit_cast(bf16x8, wf[ks * 4 + e]), d[e], 0, 0, 0);
      }
      v4u y;
#pragma unroll
      for (int e = 0; e < 4; ++e) {
        float t = ((d[e][0] + d[e][1]) + (d[e][2] + d[e][3])) * wgt;
        t = swap16_add(t, t); t = swap32_add(t, t);
        y[e] = nosent(t);
      }
      {
        const int yo = trole == 7 ? (c16 & 7) * 2048 + (c16 >> 3) * 16 : c16 * 2048;
        ST128(y, rt, lane < 16 ? yo : (int)OOB, WS_Y + (unsigned)(b * NUNIT + (trole & 3) * 16) * 2048u + (g & 63) * 32 + (trole < 4 ? 16 : 0), AUX_SC1);
      }
      STAMP(6);
      if (trole >= 4 && is_m) {
        bf16x8 a1[14];
        s16x4 a2[7];
        {
          const int q = c16 >> 2, p = c16 & 3;
          const LAS unsigned char* s1 = (const LAS unsigned char*)(smem + L_W1) + (g4 * 8 + q) * 32 + p * 8;
          const LAS unsigned char* s2 = (const LAS unsigned char*)(smem + L_W2) + (g4 * 4 + q) * (DM * 2) + (hw * 112 + p * 4) * 2;
#pragma unroll
          for (int s = 0; s < 14; ++s) {
            const bf16x4 lo = __builtin_amdgcn_ds_read_tr16_b64_v4bf16((LAS bf16x4*)(s1 + s * 1024));
            const bf16x4 hi = __builtin_amdgcn_ds_read_tr16_b64_v4bf16((LAS bf16x4*)(s1 + s * 1024 + 128));
            a1[s] = __builtin_shufflevector(lo, hi, 0, 1, 2, 3, 4, 5, 6, 7);
          }
#pragma unroll
          for (int t = 0; t < 7; ++t) a2[t] = __builtin_bit_cast(s16x4, __builtin_amdgcn_ds_read_tr16_b64_v4bf16((LAS bf16x4*)(s2 + t * 32)));
        }
        SPIN(LSPIN_CAP) { PIN(); if (tm[14] != 0u) break; __builtin_amdgcn_s_sleep(2); }
        PIN();
        v4u h0, h1;
        {
          const unsigned hb = WS_HB + (unsigned)(mi % HREP) * (NB * DM * 4) + hw * (DM * 4);
          const int vo = lane < 56 ? lane * 32 : (int)OOB;
          SPIN(SPIN_CAP) {
            PIN();
            h0 = LD128(rt, vo, hb, AUX_SC1); h1 = LD128(rt, vo, hb + 16, AUX_SC1);
            const bool ok = valid4(h0) & valid4(h1);
            if (__all(ok)) break;
          }
        }
        STAMP(7);
        {
          float v[8];
#pragma unroll
          for (int k = 0; k < 4; ++k) { v[k] = u2f(h0[k]); v[k + 4] = u2f(h1[k]); }
          ln8(v, lane);
          const v4u hn = {pk2(v[0], v[1]), pk2(v[2], v[3]), pk2(v[4], v[5]), pk2(v[6], v[7])};
          if (lane < 56) *(v4u*)(smem + L_HN + hw * HN_STRIDE + lane * 16) = hn;
          LGKM0();
          if (lane == 0) LDS_INC(13);
          SPIN(LSPIN_CAP) { PIN(); if (tm[13] >= 4u) break; }
          PIN();
        }
        f32x4 d1a = {0.f, 0.f, 0.f, 0.f}, d1b = {0.f, 0.f, 0.f, 0.f};
        {
          const unsigned char* hp = smem + L_HN + (c16 & 3) * HN_STRIDE + g4 * 16;
          bf16x8 bfr[14];
#pragma unroll
          for (int s = 0; s < 14; ++s) bfr[s] = *(const bf16x8*)(hp + s * 64);
          __builtin_amdgcn_sched_barrier(0);
#pragma unroll
          for (int s = 0; s < 14; s += 2) {
            d1a = __builtin_amdgcn_mfma_f32_16x16x32_bf16(a1[s], bfr[s], d1a, 0, 0, 0);
            d1b = __builtin_amdgcn_mfma_f32_16x16x32_bf16(a1[s + 1], bfr[s + 1], d1b, 0, 0, 0);
          }
        }
        float sv[4];
#pragma unroll
        for (int r = 0; r < 4; ++r) { const float t = rbf(d1a[r] + d1b[r]); sv[r] = t * __builtin_amdgcn_rcpf(1.0f + __builtin_amdgcn_exp2f(-t * LOG2E)); }
        const v2u hbp = {pk2(sv[0], sv[1]), pk2(sv[2], sv[3])};
        const s16x4 b2 = __builtin_bit_cast(s16x4, hbp);
        const int vo = c16 < 4 ? c16 * (NUNIT * NM * 32) + (g4 >> 1) * (NM * 32) + (g4 & 1) * 16 : (int)OOB;
        f32x4 o2[7];
#pragma unroll
        for (int t = 0; t < 7; ++t) o2[t] = __builtin_amdgcn_mfma_f32_16x16x16bf16_1k(a2[t], b2, f32x4{0.f, 0.f, 0.f, 0.f}, 0, 0, 0);
        __builtin_amdgcn_sched_barrier(0);
#pragma unroll
        for (int t = 0; t < 7; ++t) {
          const f32x4 o = o2[t];
          const v4u ov = {nosent(o[0]), nosent(o[1]), nosent(o[2]), nosent(o[3])};
          ST128(ov, rt, vo, WS_O + (unsigned)(hw * 14 + 2 * t) * (NM * 32u) + mi * 32, AUX_SC1);
        }
        STAMP(2);
      }
    } else if (unit < NUNIT) {
      const int col = unit * 8 + 4 * (lane & 1) + 2 * half + (g4 & 1);
      const float xv = u2f((unsigned)__builtin_amdgcn_raw_buffer_load_b16(rx, col * 2, b * (DM * 2), 0) << 16);
      const __amdgpu_buffer_rsrc_t rout = mkrs(out, NB * DM * 2);
      const unsigned yb = WS_Y + (unsigned)(b * NUNIT + unit) * 2048u, ob = WS_O + (unsigned)(b * NUNIT + unit) * (NM * 32u);
      v4u p0 = {SENT, SENT, SENT, SENT}, p1 = p0;
      SPIN(SPIN_CAP) {
        PIN();
        const bool k0 = valid4(p0), k1 = valid4(p1);
        if (__all(k0 & k1)) break;
        const v4u n0 = LD128(rt, k0 ? (int)OOB : lane * 16, yb, AUX_SC1), n1 = LD128(rt, k1 ? (int)OOB : lane * 16, yb + 1024, AUX_SC1);
        p0 = k0 ? p0 : n0; p1 = k1 ? p1 : n1;
      }
      STAMP(6);
      const float hv = rbf(xv + rbf(reduce_pieces(asf(p0) + asf(p1))));
      const int rep = (lane >> 1) & 7;
      ST32(nosent(hv), rt, rep < HREP ? rep * (NB * DM * 4) + col * 4 : (int)OOB, WS_HB + b * (DM * 4), AUX_SC1);
      if (lane == 0) tm[14] = 1u;
      v4u p2 = {SENT, SENT, SENT, SENT}, p3 = lane < 32 ? p2 : v4u{0u, 0u, 0u, 0u};
      p0 = p2; p1 = p2;
      __builtin_amdgcn_s_sleep(F_SLEEP);
      SPIN(SPIN_CAP) {
        PIN();
        const bool k0 = valid4(p0), k1 = valid4(p1), k2 = valid4(p2), k3 = valid4(p3);
        if (__all(k0 & k1 & k2 & k3)) break;
        const v4u n0 = LD128(rt, k0 ? (int)OOB : lane * 16, ob, AUX_SC1), n1 = LD128(rt, k1 ? (int)OOB : lane * 16, ob + 1024, AUX_SC1);
        const v4u n2 = LD128(rt, k2 ? (int)OOB : lane * 16, ob + 2048, AUX_SC1), n3 = LD128(rt, k3 ? (int)OOB : lane * 16, ob + 3072, AUX_SC1);
        p0 = k0 ? p0 : n0; p1 = k1 ? p1 : n1; p2 = k2 ? p2 : n2; p3 = k3 ? p3 : n3;
      }
      STAMP(7);
      const float ov = hv + rbf(reduce_pieces((asf(p0) + asf(p1)) + (asf(p2) + asf(p3))));
      __builtin_amdgcn_raw_buffer_store_b16(__builtin_bit_cast(u16, (__bf16)ov), rout, rep == 0 ? col * 2 : (int)OOB, b * (DM * 2), 0);
      STAMP(1);
      asm volatile(".Lfd_code_end:");
    }
  }
}

extern "C" void kernel_launch(void* const* d_in, const int* in_sizes, int n_in, void* d_out, int out_size, void* d_ws, size_t ws_size, hipStream_t stream) {
  fused_decode<<<NWG, NTHR, 0, stream>>>((const u16*)d_in[0], (const u16*)d_in[1], (const u16*)d_in[2], (const u16*)d_in[3], (const u16*)d_in[4],
                                         (const u16*)d_in[6], (const u16*)d_in[7], (u16*)d_out, (unsigned char*)d_ws);
}
```

```cpp
#define STAMPS 1
#include <cstdio>
#include <hip/hip_runtime.h>
#include <stdint.h>

typedef unsigned v4u __attribute__((ext_vector_type(4)));
typedef unsigned v2u __attribute__((ext_vector_type(2)));
typedef float f32x4 __attribute__((ext_vector_type(4)));
typedef float f32x2 __attribute__((ext_vector_type(2)));
typedef __bf16 bf16x2 __attribute__((ext_vector_type(2)));
typedef __bf16 bf16x4 __attribute__((ext_vector_type(4)));
typedef __bf16 bf16x8 __attribute__((ext_vector_type(8)));
typedef short s16x4 __attribute__((ext_vector_type(4)));
typedef unsigned short u16;
typedef unsigned long long u64;
#define LAS __attribute__((address_space(3)))

constexpr int NB = 4, DM = 448, NH = 16, SK = 8192, DH = 256, FF = 1792;
constexpr int NWG = 256, NTHR = 512;
constexpr int NL = 256;
#define WINDOW 8
constexpr int WIN = WINDOW;
constexpr int NUNIT = 56;
constexpr int NM = 112;
#define HREP 4
constexpr unsigned SENT = 0xFFFFFFFFu;
constexpr int AUX_NT = 2, AUX_SC1 = 16;
#define AUX_WQ 2
#define AUX_WO 2
#define MAPPING 1
#define NO_PREP 0
#define NO_QWAIT 0
#define VDEF_FROM 256
#define VDEF_TO 256
#define CODE_PF 1
#define PF_AT 13
#define PREP_SLEEP 100
#define F_SLEEP 24
constexpr unsigned OOB = 0x7FFFFFF0u;
constexpr float LOG2E = 1.4426950408889634f;

constexpr unsigned WS_EPOCH = 0, WS_READY = 4096, WS_ML = 8192, WS_HB = 16384, WS_Q = 1u << 17, WS_Y = 1u << 18, WS_O = 1u << 20, WS_BYTES = 2u << 20;
constexpr unsigned WS_STAMP = 64u << 20;
constexpr int L_WOF = 0;
constexpr int L_W1 = L_WOF + 3 * 32 * 1024;
constexpr int L_W2 = L_W1 + 14336;
constexpr int L_SC = L_W2 + 14336;
constexpr int L_QB = L_SC + 8192;
constexpr int L_OL = L_QB + 512;
constexpr int OL_STRIDE = 528;
constexpr int L_HN = L_OL + 16 * OL_STRIDE;
constexpr int HN_STRIDE = 912;
constexpr int L_MISC = L_HN + 4 * HN_STRIDE;
constexpr int L_BYTES = L_MISC + 128;
static_assert(L_BYTES <= 163840 && L_MISC % 16 == 0 && L_HN % 16 == 0, "lds");

__device__ __forceinline__ float u2f(unsigned u) { return __builtin_bit_cast(float, u); }
__device__ __forceinline__ unsigned f2u(float f) { return __builtin_bit_cast(unsigned, f); }
__device__ __forceinline__ float rbf(float f) { return (float)(__bf16)f; }
__device__ __forceinline__ unsigned pk2(float lo, float hi) { bf16x2 v = {(__bf16)lo, (__bf16)hi}; return __builtin_bit_cast(unsigned, v); }
__device__ __forceinline__ float blo(unsigned u) { return u2f(u << 16); }
__device__ __forceinline__ float bhi(unsigned u) { return u2f(u & 0xffff0000u); }
__device__ __forceinline__ float dot2(unsigned a, unsigned b, float c) {
  return __builtin_amdgcn_fdot2_f32_bf16(__builtin_bit_cast(bf16x2, a), __builtin_bit_cast(bf16x2, b), c, false);
}
template <int CTRL> __device__ __forceinline__ float dpp(float x) {
  return __builtin_bit_cast(float, __builtin_amdgcn_mov_dpp(__builtin_bit_cast(int, x), CTRL, 0xf, 0xf, true));
}
constexpr int XOR1 = 0xB1, XOR2 = 0x4E, XOR7 = 0x141, ROR8 = 0x128, ROR4 = 0x124;
__device__ __forceinline__ float swap16_add(float a, float b) {
  auto s = __builtin_amdgcn_permlane16_swap(f2u(a), f2u(b), false, false);
  const unsigned s0 = s[0], s1 = s[1];
  return u2f(s0) + u2f(s1);
}
__device__ __forceinline__ float swap32_add(float a, float b) {
  auto s = __builtin_amdgcn_permlane32_swap(f2u(a), f2u(b), false, false);
  const unsigned s0 = s[0], s1 = s[1];
  return u2f(s0) + u2f(s1);
}
__device__ __forceinline__ float swap16_max(float a) {
  auto s = __builtin_amdgcn_permlane16_swap(f2u(a), f2u(a), false, false);
  const unsigned s0 = s[0], s1 = s[1];
  return fmaxf(u2f(s0), u2f(s1));
}
__device__ __forceinline__ float swap32_max(float a) {
  auto s = __builtin_amdgcn_permlane32_swap(f2u(a), f2u(a), false, false);
  const unsigned s0 = s[0], s1 = s[1];
  return fmaxf(u2f(s0), u2f(s1));
}
__device__ __forceinline__ float row_sum(float x) { x += dpp<ROR8>(x); x += dpp<ROR4>(x); x += dpp<XOR2>(x); x += dpp<XOR1>(x); return x; }
__device__ __forceinline__ float row_max(float x) { x = fmaxf(x, dpp<ROR8>(x)); x = fmaxf(x, dpp<ROR4>(x)); x = fmaxf(x, dpp<XOR2>(x)); x = fmaxf(x, dpp<XOR1>(x)); return x; }
__device__ __forceinline__ float wave_sum(float x) { x = row_sum(x); x = swap16_add(x, x); return swap32_add(x, x); }
__device__ __forceinline__ float wave_max(float x) { x = row_max(x); x = swap16_max(x); return swap32_max(x); }
__device__ __forceinline__ unsigned nosent(float f) { const unsigned u = f2u(f); return u < 0xFFFFFFFEu ? u : 0xFFFFFFFEu; }

__device__ __forceinline__ __amdgpu_buffer_rsrc_t mkrs(const void* p, unsigned bytes) { return __builtin_amdgcn_make_buffer_rsrc((void*)p, 0, bytes, 0x00020000); }
#define LD128(rs, vo, so, aux) __builtin_amdgcn_raw_buffer_load_b128(rs, vo, so, aux)
#define LD64(rs, vo, so, aux) __builtin_amdgcn_raw_buffer_load_b64(rs, vo, so, aux)
#define ST128(v, rs, vo, so, aux) __builtin_amdgcn_raw_buffer_store_b128(v, rs, vo, so, aux)
#define ST64(v, rs, vo, so, aux) __builtin_amdgcn_raw_buffer_store_b64(v, rs, vo, so, aux)
#define ST32(v, rs, vo, so, aux) __builtin_amdgcn_raw_buffer_store_b32(v, rs, vo, so, aux)
#define PIN() asm volatile("" ::: "memory")
#define USE(x) asm volatile("" :: "v"(x) : "memory")
#define REDEF(x) asm volatile("" : "+v"(x) :: "memory")
#define BARRIER() asm volatile("s_waitcnt lgkmcnt(0)\n\ts_barrier" ::: "memory")
#define DRAIN() asm volatile("s_waitcnt vmcnt(0)" ::: "memory")
#define LGKM0() asm volatile("s_waitcnt lgkmcnt(0)" ::: "memory")
constexpr int SPIN_CAP = 1 << 16;
constexpr int LSPIN_CAP = 1 << 20;

#define STAMP(i) do { if (lane == 0) { const u64 t_ = __builtin_amdgcn_s_memrealtime(); *(u64*)(ws + WS_STAMP + (size_t)g * 512 + wave * 64 + (i) * 8) = t_; } } while (0)

__device__ __forceinline__ void ln8(float (&v)[8], int lane) {
  float s = ((v[0] + v[1]) + (v[2] + v[3])) + ((v[4] + v[5]) + (v[6] + v[7]));
  const float mean = wave_sum(s) * (1.0f / DM);
  float q = 0.f;
#pragma unroll
  for (int e = 0; e < 8; ++e) { const float d = v[e] - mean; q = fmaf(d, d, q); }
  q = lane < 56 ? q : 0.f;
  const float var = wave_sum(q) * (1.0f / DM);
  const float mb = rbf(mean);
  const float den = rbf(__builtin_amdgcn_sqrtf(rbf(rbf(var) + 1e-5f)));
  const float inv = __builtin_amdgcn_rcpf(den);
#pragma unroll
  for (int e = 0; e < 8; ++e) v[e] = rbf(v[e] - mb) * inv;
}
__device__ __forceinline__ float reduce_pieces(f32x4 v) {
  const float a = swap32_add(v[0], v[2]), b = swap32_add(v[1], v[3]);
  float c = swap16_add(a, b);
  c += dpp<ROR8>(c); c += dpp<ROR4>(c); c += dpp<XOR2>(c);
  return c;
}
__device__ __forceinline__ bool valid4(v4u a) { const unsigned m0 = a[0] > a[1] ? a[0] : a[1], m1 = a[2] > a[3] ? a[2] : a[3]; return (m0 > m1 ? m0 : m1) != SENT; }
__device__ __forceinline__ f32x4 asf(v4u a) { return __builtin_bit_cast(f32x4, a); }

#define SPIN(cap) _Pragma("unroll 1") for (int spin = 0; spin < (cap); ++spin)
#define LDS_INC(w) __hip_atomic_fetch_add((LAS unsigned*)(smem + L_MISC) + (w), 1u, __ATOMIC_RELAXED, __HIP_MEMORY_SCOPE_WORKGROUP)

__global__ void __launch_bounds__(NTHR, 2)
fused_decode(const u16* x, const u16* Kc, const u16* Vc, const u16* wq, const u16* wo, const u16* w1, const u16* w2, u16* out, unsigned char* ws) {
  __shared__ __attribute__((aligned(16))) unsigned char smem[L_BYTES];
  const int tid = threadIdx.x, lane = tid & 63;
  int wave = __builtin_amdgcn_readfirstlane(tid >> 6);
  const int g = ((blockIdx.x >> 6) << 6) | (((blockIdx.x & 7) + 8 * ((blockIdx.x >> 3) & 1)) << 2) | ((blockIdx.x >> 4) & 3);
  const int b = g >> 6, h = (g >> 2) & 15;
  const int half = lane >> 5, c32 = lane & 31, g4 = lane >> 4, c16 = lane & 15;
  volatile LAS unsigned* misc = (volatile LAS unsigned*)(smem + L_MISC);
  const __amdgpu_buffer_rsrc_t rws = mkrs(ws, WS_BYTES);
  const __amdgpu_buffer_rsrc_t rk = mkrs(Kc + (size_t)g * (2048 * DH), 2048 * DH * 2);
  const __amdgpu_buffer_rsrc_t rx = mkrs(x, NB * DM * 2);
  const int voff = tid * 16;
  const int hw = wave - 4;
  v4u kb[32];
  u64 pc0;
  asm volatile("s_getpc_b64 %0\n.Lfd_code_begin:" : "=s"(pc0));
  STAMP(0);
  if (tid == 0) *(u64*)(ws + WS_STAMP + (size_t)g * 512 + 7 * 8) = 1 + ((unsigned)__builtin_amdgcn_s_getreg(20 | (3 << 11)) & 0xfu);
  if (wave < 4) {
#pragma unroll
    for (int i = 0; i < WIN; ++i) kb[i] = LD128(rk, voff, i * 4096, AUX_NT);
  } else {
    const int j = b * 4 + (g & 3);
    const __amdgpu_buffer_rsrc_t rq = mkrs(wq + h * DH + j * 16, (DM - 1) * (NH * DH * 2) + 32);
    kb[0] = LD128(rx, lane < 56 ? lane * 16 : (int)OOB, hw * (DM * 2), 0);
#pragma unroll
    for (int i = 0; i < 4; ++i) kb[1 + i] = LD128(rq, (lane >> 1) * (NH * DH * 2) + (lane & 1) * 16, (hw + 4 * i) * 32 * (NH * DH * 2), AUX_WQ);
    kb[5] = LD128(rws, 0, WS_EPOCH, AUX_SC1);
#pragma unroll
    for (int i = 6; i < WIN; ++i) asm volatile("" : "=v"(kb[i]));
    if (tid == 256) { misc[12] = 0u; misc[13] = 0u; misc[14] = 0u; misc[15] = 0u; misc[16] = 0u; }
  }
  BARRIER();
  asm volatile("" : "+s"(wave));

  const int unit = g & 63;
  const bool is_m = (g & 15) < 7;
  const int mi = (g >> 4) * 7 + (g & 15);
  v4u wf[32];
  {
    const int trole = wave;
    if (wave < 4) {
      const __amdgpu_buffer_rsrc_t rv = mkrs(Vc + (size_t)g * (2048 * DH), 2048 * DH * 2);
#pragma unroll
      for (int i = WIN; i < 32; ++i) { USE(kb[i - WIN]); kb[i] = LD128(rk, voff, i * 4096, AUX_NT); PIN(); }
      STAMP(1);
      BARRIER();
      STAMP(2);
      const v4u qv = *(const v4u*)(smem + L_QB + c32 * 16);
      float* sc = (float*)(smem + L_SC) + (wave * 2 + half) * 256;
      float m = -INFINITY;
#define DOT(i) do { float d_ = dot2(kb[i][0], qv[0], 0.f); d_ = dot2(kb[i][1], qv[1], d_); d_ = dot2(kb[i][2], qv[2], d_); d_ = dot2(kb[i][3], qv[3], d_); \
                    asm volatile("" : "+v"(d_)); xs[i] = d_; } while (0)
#define TR_STEP(HALF, CTRL, BIT) _Pragma("unroll") for (int hh = 0; hh < (HALF); ++hh) { \
      const float lo_ = xs[hh], hi_ = xs[hh + (HALF)]; const float t1_ = lo_ + dpp<CTRL>(lo_), t2_ = hi_ + dpp<CTRL>(hi_); \
      xs[hh] = (c32 & (BIT)) ? t2_ : t1_; }
#define REDUCE(grp) do { \
      _Pragma("unroll") for (int i = 0; i < 16; ++i) xs[i] = swap16_add(xs[i], xs[i + 16]); \
      TR_STEP(8, ROR8, 8) TR_STEP(4, XOR7, 4) TR_STEP(2, XOR2, 2) TR_STEP(1, XOR1, 1) \
      const float s_ = rbf(xs[0]) * 0.0625f; m = fmaxf(m, s_); sc[(grp) * 32 + c32] = s_; } while (0)
      {
        float xs[32];
#pragma unroll
        for (int i = 0; i < 32; ++i) {
          REDEF(kb[i]);
          DOT(i);
          if (i < WIN) { PIN(); kb[i] = LD128(rk, voff, (32 + i) * 4096, AUX_NT); PIN(); }
        }
        REDUCE(0);
      }
#pragma unroll 1
      for (int gi = 1; gi < 8; ++gi) {
        float xs[32];
        const __amdgpu_buffer_rsrc_t rn = gi < 7 ? rk : rv;
        const int offn = gi < 7 ? (gi + 1) * 32 * 4096 : 0;
#pragma unroll
        for (int i = 0; i < 32; ++i) {
          USE(kb[i]);
          if (i + WIN < 32) kb[i + WIN] = LD128(rk, voff, (gi * 32 + i + WIN) * 4096, AUX_NT);
          else kb[i + WIN - 32] = LD128(rn, voff, offn + (i + WIN - 32) * 4096, AUX_NT);
          REDEF(kb[i]);
          DOT(i);
        }
        REDUCE(gi);
      }
      STAMP(3);
      m = wave_max(m);
      {
        LGKM0();
        f32x4* ps = (f32x4*)(smem + L_SC) + wave * 128 + lane * 2;
        f32x4 a = ps[0], bq = ps[1];
        const float mm = m * LOG2E;
#pragma unroll
        for (int e = 0; e < 4; ++e) { a[e] = __builtin_amdgcn_exp2f(fmaf(a[e], LOG2E, -mm)); bq[e] = __builtin_amdgcn_exp2f(fmaf(bq[e], LOG2E, -mm)); }
        ps[0] = a; ps[1] = bq;
        float l = ((a[0] + a[1]) + (a[2] + a[3])) + ((bq[0] + bq[1]) + (bq[2] + bq[3]));
        l = wave_sum(l);
        if (lane == 0) { misc[2 * wave] = f2u(m); misc[2 * wave + 1] = f2u(l); }
        LGKM0();
        if (lane == 0) LDS_INC(12);
      }
      float acc[8];
#pragma unroll
      for (int k = 0; k < 8; ++k) acc[k] = 0.f;
#define VLOOP(AUXV) \
_Pragma("unroll 1") \
      for (int gi = 0; gi < NL / 16; ++gi) { \
        f32x4 p4[4]; \
_Pragma("unroll") \
        for (int i = 0; i < 4; ++i) p4[i] = *(const f32x4*)(sc + gi * 16 + i * 4); \
        if (gi == PF_AT && tid == 0) misc[16] = 1u; \
_Pragma("unroll") \
        for (int i = 0; i < 16; ++i) { \
          USE(kb[i]); \
          kb[(i + WIN) & 15] = LD128(rv, voff, (gi * 16 + i + WIN) * 4096, AUXV); \
          REDEF(kb[i]); \
          const float p = p4[i >> 2][i & 3]; \
_Pragma("unroll") \
          for (int k = 0; k < 4; ++k) { acc[2 * k] = fmaf(p, blo(kb[i][k]), acc[2 * k]); acc[2 * k + 1] = fmaf(p, bhi(kb[i][k]), acc[2 * k + 1]); } \
          asm volatile("" : "+v"(acc[0]), "+v"(acc[1]), "+v"(acc[2]), "+v"(acc[3]), "+v"(acc[4]), "+v"(acc[5]), "+v"(acc[6]), "+v"(acc[7])); \
        } \
      } \

      if (g >= VDEF_FROM && g < VDEF_TO) { VLOOP(0) } else { VLOOP(AUX_NT) }
      STAMP(4);
      {
        v4u hi, lo;
#pragma unroll
        for (int k = 0; k < 4; ++k) {
          hi[k] = pk2(acc[2 * k], acc[2 * k + 1]);
          lo[k] = pk2(acc[2 * k] - blo(hi[k]), acc[2 * k + 1] - bhi(hi[k]));
        }
        unsigned char* ol = smem + L_OL + (wave * 4 + half * 2) * OL_STRIDE + c32 * 16;
        *(v4u*)ol = hi; *(v4u*)(ol + OL_STRIDE) = lo;
      }
      BARRIER();
      if (wave < 3) {
        const v4u* src = (const v4u*)(smem + L_WOF + wave * 32768) + lane;
#pragma unroll
        for (int i = 0; i < 32; ++i) wf[i] = src[i * 64];
      }
    } else {
      {
        float v[8];
#pragma unroll
        for (int k = 0; k < 4; ++k) { v[2 * k] = blo(kb[0][k]); v[2 * k + 1] = bhi(kb[0][k]); }
        ln8(v, lane);
        const v4u xn = {pk2(v[0], v[1]), pk2(v[2], v[3]), pk2(v[4], v[5]), pk2(v[6], v[7])};
        if (lane < 56) *(v4u*)(smem + L_HN + hw * HN_STRIDE + lane * 16) = xn;
#pragma unroll
        for (int i = 0; i < 4; ++i) if (hw + 4 * i < 14) *(v4u*)(smem + L_W1 + (hw + 4 * i) * 1024 + lane * 16) = kb[1 + i];
        LGKM0();
        if (lane == 0) LDS_INC(15);
      }
      const unsigned e0 = __builtin_amdgcn_readfirstlane(kb[5][0]), e1 = __builtin_amdgcn_readfirstlane(kb[5][1]);
      v2u tagv, nextep;
      unsigned qtag = e0 + 0x9E3779B9u;
      qtag ^= qtag >> 16; qtag *= 0x85EBCA6Bu; qtag ^= qtag >> 13; qtag *= 0xC2B2AE35u; qtag ^= qtag >> 16;
      {
        u64 tag = ((u64)e1 << 32) | e0;
        const u64 ne = tag + 1;
        tag += 0x9E3779B97F4A7C15ull; tag = (tag ^ (tag >> 30)) * 0xBF58476D1CE4E5B9ull; tag = (tag ^ (tag >> 27)) * 0x94D049BB133111EBull; tag ^= tag >> 31;
        tagv = v2u{(unsigned)tag, (unsigned)(tag >> 32)};
        nextep = v2u{(unsigned)ne, (unsigned)(ne >> 32)};
      }
      if (hw == 0) {
        SPIN(LSPIN_CAP) { PIN(); if (misc[15] >= 4u) break; }
        PIN();
        const int q = c16 >> 2, p = c16 & 3;
        const LAS unsigned char* s1 = (const LAS unsigned char*)(smem + L_W1) + (g4 * 8 + q) * 32 + p * 8;
        const unsigned char* hp = smem + L_HN + (c16 & 3) * HN_STRIDE + g4 * 16;
        bf16x8 a1[14], bfr[14];
#pragma unroll
        for (int s = 0; s < 14; ++s) {
          const bf16x4 lo = __builtin_amdgcn_ds_read_tr16_b64_v4bf16((LAS bf16x4*)(s1 + s * 1024));
          const bf16x4 hi = __builtin_amdgcn_ds_read_tr16_b64_v4bf16((LAS bf16x4*)(s1 + s * 1024 + 128));
          a1[s] = __builtin_shufflevector(lo, hi, 0, 1, 2, 3, 4, 5, 6, 7);
          bfr[s] = *(const bf16x8*)(hp + s * 64);
        }
        __builtin_amdgcn_sched_barrier(0);
        f32x4 da = {0.f, 0.f, 0.f, 0.f}, db = {0.f, 0.f, 0.f, 0.f};
#pragma unroll
        for (int s = 0; s < 14; s += 2) {
          da = __builtin_amdgcn_mfma_f32_16x16x32_bf16(a1[s], bfr[s], da, 0, 0, 0);
          db = __builtin_amdgcn_mfma_f32_16x16x32_bf16(a1[s + 1], bfr[s + 1], db, 0, 0, 0);
        }
        da += db;
        const v4u qg = {pk2(da[0], da[1]), qtag, pk2(da[2], da[3]), qtag};
        ST128(qg, rws, c16 < 4 ? c16 * 1024 + g4 * 16 : (int)OOB, WS_Q + h * 4096 + (b * 4 + (g & 3)) * 64, AUX_SC1);
        STAMP(1);
      } else if (hw == 1) {
        v4u qg;
        SPIN(SPIN_CAP) {
          PIN();
          qg = LD128(rws, lane * 16, WS_Q + h * 4096 + b * 1024, AUX_SC1);
          if (NO_QWAIT || __all(qg[1] == qtag && qg[3] == qtag)) break;
        }
        *(v2u*)(smem + L_QB + lane * 8) = v2u{qg[0], qg[2]};
        STAMP(1);
      }
      BARRIER();
      if (hw == 0) {
        const v4u s4 = {SENT, SENT, SENT, SENT};
        if (unit < NUNIT) {
          const unsigned yb = WS_Y + (unsigned)(b * NUNIT + unit) * 2048u, ob = WS_O + (unsigned)(b * NUNIT + unit) * (NM * 32u);
          ST128(s4, rws, lane * 16, yb, AUX_SC1); ST128(s4, rws, lane * 16, yb + 1024, AUX_SC1);
          ST128(s4, rws, lane * 16, ob, AUX_SC1); ST128(s4, rws, lane * 16, ob + 1024, AUX_SC1); ST128(s4, rws, lane * 16, ob + 2048, AUX_SC1);
          ST128(s4, rws, lane < 32 ? lane * 16 : (int)OOB, ob + 3072, AUX_SC1);
          ST128(s4, rws, lane < 2 * HREP ? (lane >> 1) * (NB * DM * 4) + (lane & 1) * 16 : (int)OOB, WS_HB + (b * DM + unit * 8) * 4, AUX_SC1);
        }
        ST128(s4, rws, lane < 2 ? lane * 16 : (int)OOB, WS_ML + g * 32, AUX_SC1);
        DRAIN();
        ST64(tagv, rws, lane == 0 ? 0 : (int)OOB, WS_READY + g * 8, AUX_SC1);
      }
      if (is_m && !NO_PREP) {
        const __amdgpu_buffer_rsrc_t r1 = mkrs(w1 + mi * 16, (DM - 1) * (FF * 2) + 32);
        const __amdgpu_buffer_rsrc_t r2 = mkrs(w2 + (size_t)mi * 16 * DM, 16 * DM * 2);
        v4u t1[4], t2[4];
#pragma unroll
        for (int i = 0; i < 4; ++i) {
          t1[i] = LD128(r1, (lane >> 1) * (FF * 2) + (lane & 1) * 16, (hw + 4 * i) * 32 * (FF * 2), AUX_NT);
          t2[i] = LD128(r2, lane * 16, (hw + 4 * i) * 1024, AUX_NT);
        }
#pragma unroll
        for (int i = 0; i < 4; ++i) if (hw + 4 * i < 14) {
          *(v4u*)(smem + L_W1 + (hw + 4 * i) * 1024 + lane * 16) = t1[i];
          *(v4u*)(smem + L_W2 + (hw + 4 * i) * 1024 + lane * 16) = t2[i];
        }
      }
      const __amdgpu_buffer_rsrc_t ro = mkrs(wo + (size_t)h * DH * DM, NO_PREP ? 0 : DH * DM * 2);
      const int ooff = g4 * 8 * (DM * 2) + (hw < 3 ? c16 : (c16 & 7)) * 16;
      const bool sel = hw == 3 && c16 >= 8;
      v4u* dst = (v4u*)(smem + L_WOF + hw * 32768) + lane;
#pragma unroll
      for (int ks = 0; ks < 8; ++ks) {
        __builtin_amdgcn_s_sleep(PREP_SLEEP);
        PIN();
        v4u r[8];
#pragma unroll
        for (int jj = 0; jj < 8; ++jj) r[jj] = LD128(ro, ooff, (ks * 32 + jj) * (DM * 2) + hw * 256, AUX_WO);
        if (hw < 3) {
#pragma unroll
          for (int e = 4; e < 8; ++e) {
            v4u t;
#pragma unroll
            for (int d = 0; d < 4; ++d) t[d] = __builtin_amdgcn_perm(r[2 * d + 1][e >> 1], r[2 * d][e >> 1], (e & 1) ? 0x07060302u : 0x05040100u);
            dst[(ks * 4 + e - 4) * 64] = t;
          }
        }
#pragma unroll
        for (int jj = 0; jj < 8; ++jj) { r[jj][0] = sel ? r[jj][2] : r[jj][0]; r[jj][1] = sel ? r[jj][3] : r[jj][1]; }
#pragma unroll
        for (int e = 0; e < 4; ++e)
#pragma unroll
          for (int d = 0; d < 4; ++d)
            wf[ks * 4 + e][d] = __builtin_amdgcn_perm(r[2 * d + 1][e >> 1], r[2 * d][e >> 1], (e & 1) ? 0x07060302u : 0x05040100u);
        asm volatile("" : "+v"(wf[ks * 4]), "+v"(wf[ks * 4 + 1]), "+v"(wf[ks * 4 + 2]), "+v"(wf[ks * 4 + 3]) :: "memory");
      }
      STAMP(3);
      if (hw == 0) {
        SPIN(SPIN_CAP) {
          PIN();
          const v4u r0 = LD128(rws, lane * 16, WS_READY, AUX_SC1), r1 = LD128(rws, lane * 16, WS_READY + 1024, AUX_SC1);
          const bool ok = r0[0] == tagv[0] && r0[1] == tagv[1] && r0[2] == tagv[0] && r0[3] == tagv[1] && r1[0] == tagv[0] && r1[1] == tagv[1] && r1[2] == tagv[0] && r1[3] == tagv[1];
          if (__all(ok)) break;
          __builtin_amdgcn_s_sleep(16);
        }
        ST64(nextep, rws, (g == 0 && lane == 0) ? 0 : (int)OOB, WS_EPOCH, AUX_SC1);
        SPIN(LSPIN_CAP) { PIN(); if (misc[12] >= 4u) break; __builtin_amdgcn_s_sleep(16); }
        PIN();
        const float mo = u2f(misc[2 * (lane & 3)]);
        const v2u mlo = {nosent(mo), nosent(u2f(misc[2 * (lane & 3) + 1]))};
        ST64(mlo, rws, lane < 4 ? lane * 8 : (int)OOB, WS_ML + g * 32, AUX_SC1);
        v2u ml;
        SPIN(SPIN_CAP) {
          PIN();
          ml = LD64(rws, c16 * 8, WS_ML + (g >> 2) * 128, AUX_SC1);
          if (__all(ml[0] != SENT && ml[1] != SENT)) break;
          __builtin_amdgcn_s_sleep(16);
        }
        const float M = row_max(u2f(ml[0]));
        const float L = row_sum(u2f(ml[1]) * __builtin_amdgcn_exp2f((u2f(ml[0]) - M) * LOG2E));
        if (lane < 4) misc[8 + lane] = f2u(__builtin_amdgcn_exp2f((mo - M) * LOG2E) / L);
      }
      if (hw == 1) {
        SPIN(LSPIN_CAP) { PIN(); if (misc[16] != 0u) break; __builtin_amdgcn_s_sleep(32); }
        unsigned len;
        asm volatile("s_mov_b32 %0, .Lfd_code_end-.Lfd_code_begin" : "=s"(len));
        const __amdgpu_buffer_rsrc_t rc = mkrs((const void*)pc0, len);
        const int cbase = len > 16384u ? (int)(len - 16384u) : 0;
        v4u t[16];
#pragma unroll
        for (int i = 0; i < 16; ++i) t[i] = LD128(rc, lane * 16, cbase + i * 1024, 0);
        const unsigned dummy = __builtin_amdgcn_raw_buffer_load_b16(mkrs(out, NB * DM * 2), 0, 0, 0);
#pragma unroll
        for (int i = 0; i < 16; ++i) USE(t[i]);
        USE(dummy);
      }
      STAMP(4);
      BARRIER();
    }
    STAMP(5);
    const __amdgpu_buffer_rsrc_t rt = rws;
    volatile LAS unsigned* tm = misc;
    if (trole != 3) {
      const unsigned char* ol = smem + L_OL + c16 * OL_STRIDE + g4 * 16;
      const float wgt = u2f(misc[8 + g4]);
      f32x4 d[4];
#pragma unroll
      for (int e = 0; e < 4; ++e) d[e] = f32x4{0.f, 0.f, 0.f, 0.f};
      bf16x8 af[8];
#pragma unroll
      for (int ks = 0; ks < 8; ++ks) af[ks] = *(const bf16x8*)(ol + ks * 64);
      __builtin_amdgcn_sched_barrier(0);
#pragma unroll
      for (int ks = 0; ks < 8; ++ks) {
#pragma unroll
        for (int e = 0; e < 4; ++e) d[e] = __builtin_amdgcn_mfma_f32_16x16x32_bf16(af[ks], __builtin_bit_cast(bf16x8, wf[ks * 4 + e]), d[e], 0, 0, 0);
      }
      v4u y;
#pragma unroll
      for (int e = 0; e < 4; ++e) {
        float t = ((d[e][0] + d[e][1]) + (d[e][2] + d[e][3])) * wgt;
        t = swap16_add(t, t); t = swap32_add(t, t);
        y[e] = nosent(t);
      }
      {
        const int yo = trole == 7 ? (c16 & 7) * 2048 + (c16 >> 3) * 16 : c16 * 2048;
        ST128(y, rt, lane < 16 ? yo : (int)OOB, WS_Y + (unsigned)(b * NUNIT + (trole & 3) * 16) * 2048u + (g & 63) * 32 + (trole < 4 ? 16 : 0), AUX_SC1);
      }
      STAMP(6);
      if (trole >= 4 && is_m) {
        bf16x8 a1[14];
        s16x4 a2[7];
        {
          const int q = c16 >> 2, p = c16 & 3;
          const LAS unsigned char* s1 = (const LAS unsigned char*)(smem + L_W1) + (g4 * 8 + q) * 32 + p * 8;
          const LAS unsigned char* s2 = (const LAS unsigned char*)(smem + L_W2) + (g4 * 4 + q) * (DM * 2) + (hw * 112 + p * 4) * 2;
#pragma unroll
          for (int s = 0; s < 14; ++s) {
            const bf16x4 lo = __builtin_amdgcn_ds_read_tr16_b64_v4bf16((LAS bf16x4*)(s1 + s * 1024));
            const bf16x4 hi = __builtin_amdgcn_ds_read_tr16_b64_v4bf16((LAS bf16x4*)(s1 + s * 1024 + 128));
            a1[s] = __builtin_shufflevector(lo, hi, 0, 1, 2, 3, 4, 5, 6, 7);
          }
#pragma unroll
          for (int t = 0; t < 7; ++t) a2[t] = __builtin_bit_cast(s16x4, __builtin_amdgcn_ds_read_tr16_b64_v4bf16((LAS bf16x4*)(s2 + t * 32)));
        }
        SPIN(LSPIN_CAP) { PIN(); if (tm[14] != 0u) break; __builtin_amdgcn_s_sleep(2); }
        PIN();
        v4u h0, h1;
        {
          const unsigned hb = WS_HB + (unsigned)(mi % HREP) * (NB * DM * 4) + hw * (DM * 4);
          const int vo = lane < 56 ? lane * 32 : (int)OOB;
          SPIN(SPIN_CAP) {
            PIN();
            h0 = LD128(rt, vo, hb, AUX_SC1); h1 = LD128(rt, vo, hb + 16, AUX_SC1);
            const bool ok = valid4(h0) & valid4(h1);
            if (__all(ok)) break;
          }
        }
        STAMP(7);
        {
          float v[8];
#pragma unroll
          for (int k = 0; k < 4; ++k) { v[k] = u2f(h0[k]); v[k + 4] = u2f(h1[k]); }
          ln8(v, lane);
          const v4u hn = {pk2(v[0], v[1]), pk2(v[2], v[3]), pk2(v[4], v[5]), pk2(v[6], v[7])};
          if (lane < 56) *(v4u*)(smem + L_HN + hw * HN_STRIDE + lane * 16) = hn;
          LGKM0();
          if (lane == 0) LDS_INC(13);
          SPIN(LSPIN_CAP) { PIN(); if (tm[13] >= 4u) break; }
          PIN();
        }
        f32x4 d1a = {0.f, 0.f, 0.f, 0.f}, d1b = {0.f, 0.f, 0.f, 0.f};
        {
          const unsigned char* hp = smem + L_HN + (c16 & 3) * HN_STRIDE + g4 * 16;
          bf16x8 bfr[14];
#pragma unroll
          for (int s = 0; s < 14; ++s) bfr[s] = *(const bf16x8*)(hp + s * 64);
          __builtin_amdgcn_sched_barrier(0);
#pragma unroll
          for (int s = 0; s < 14; s += 2) {
            d1a = __builtin_amdgcn_mfma_f32_16x16x32_bf16(a1[s], bfr[s], d1a, 0, 0, 0);
            d1b = __builtin_amdgcn_mfma_f32_16x16x32_bf16(a1[s + 1], bfr[s + 1], d1b, 0, 0, 0);
          }
        }
        float sv[4];
#pragma unroll
        for (int r = 0; r < 4; ++r) { const float t = rbf(d1a[r] + d1b[r]); sv[r] = t * __builtin_amdgcn_rcpf(1.0f + __builtin_amdgcn_exp2f(-t * LOG2E)); }
        const v2u hbp = {pk2(sv[0], sv[1]), pk2(sv[2], sv[3])};
        const s16x4 b2 = __builtin_bit_cast(s16x4, hbp);
        const int vo = c16 < 4 ? c16 * (NUNIT * NM * 32) + (g4 >> 1) * (NM * 32) + (g4 & 1) * 16 : (int)OOB;
        f32x4 o2[7];
#pragma unroll
        for (int t = 0; t < 7; ++t) o2[t] = __builtin_amdgcn_mfma_f32_16x16x16bf16_1k(a2[t], b2, f32x4{0.f, 0.f, 0.f, 0.f}, 0, 0, 0);
        __builtin_amdgcn_sched_barrier(0);
#pragma unroll
        for (int t = 0; t < 7; ++t) {
          const f32x4 o = o2[t];
          const v4u ov = {nosent(o[0]), nosent(o[1]), nosent(o[2]), nosent(o[3])};
          ST128(ov, rt, vo, WS_O + (unsigned)(hw * 14 + 2 * t) * (NM * 32u) + mi * 32, AUX_SC1);
        }
        STAMP(2);
      }
    } else if (unit < NUNIT) {
      const int col = unit * 8 + 4 * (lane & 1) + 2 * half + (g4 & 1);
      const float xv = u2f((unsigned)__builtin_amdgcn_raw_buffer_load_b16(rx, col * 2, b * (DM * 2), 0) << 16);
      const __amdgpu_buffer_rsrc_t rout = mkrs(out, NB * DM * 2);
      const unsigned yb = WS_Y + (unsigned)(b * NUNIT + unit) * 2048u, ob = WS_O + (unsigned)(b * NUNIT + unit) * (NM * 32u);
      v4u p0 = {SENT, SENT, SENT, SENT}, p1 = p0;
      SPIN(SPIN_CAP) {
        PIN();
        const bool k0 = valid4(p0), k1 = valid4(p1);
        if (__all(k0 & k1)) break;
        const v4u n0 = LD128(rt, k0 ? (int)OOB : lane * 16, yb, AUX_SC1), n1 = LD128(rt, k1 ? (int)OOB : lane * 16, yb + 1024, AUX_SC1);
        p0 = k0 ? p0 : n0; p1 = k1 ? p1 : n1;
      }
      STAMP(6);
      const float hv = rbf(xv + rbf(reduce_pieces(asf(p0) + asf(p1))));
      const int rep = (lane >> 1) & 7;
      ST32(nosent(hv), rt, rep < HREP ? rep * (NB * DM * 4) + col * 4 : (int)OOB, WS_HB + b * (DM * 4), AUX_SC1);
      if (lane == 0) tm[14] = 1u;
      v4u p2 = {SENT, SENT, SENT, SENT}, p3 = lane < 32 ? p2 : v4u{0u, 0u, 0u, 0u};
      p0 = p2; p1 = p2;
      __builtin_amdgcn_s_sleep(F_SLEEP);
      SPIN(SPIN_CAP) {
        PIN();
        const bool k0 = valid4(p0), k1 = valid4(p1), k2 = valid4(p2), k3 = valid4(p3);
        if (__all(k0 & k1 & k2 & k3)) break;
        const v4u n0 = LD128(rt, k0 ? (int)OOB : lane * 16, ob, AUX_SC1), n1 = LD128(rt, k1 ? (int)OOB : lane * 16, ob + 1024, AUX_SC1);
        const v4u n2 = LD128(rt, k2 ? (int)OOB : lane * 16, ob + 2048, AUX_SC1), n3 = LD128(rt, k3 ? (int)OOB : lane * 16, ob + 3072, AUX_SC1);
        p0 = k0 ? p0 : n0; p1 = k1 ? p1 : n1; p2 = k2 ? p2 : n2; p3 = k3 ? p3 : n3;
      }
      STAMP(7);
      const float ov = hv + rbf(reduce_pieces((asf(p0) + asf(p1)) + (asf(p2) + asf(p3))));
      __builtin_amdgcn_raw_buffer_store_b16(__builtin_bit_cast(u16, (__bf16)ov), rout, rep == 0 ? col * 2 : (int)OOB, b * (DM * 2), 0);
      STAMP(1);
      asm volatile(".Lfd_code_end:");
    }
  }
}

__global__ void dbg_print(unsigned char* ws) {
  if (threadIdx.x != 0) return;
  const u64* st = (const u64*)(ws + WS_STAMP);
  u64 t0 = ~0ull;
  for (int g = 0; g < 256; ++g) for (int w = 0; w < 8; ++w) t0 = st[g * 64 + w * 8] < t0 ? st[g * 64 + w * 8] : t0;
  const int idx[6][2] = {{0, 1}, {0, 2}, {0, 3}, {0, 4}, {3, 6}, {3, 7}};
  const char* nm[6] = {"prefetch", "bar1", "Kdone", "Vdone", "Rfull", "Ffull"};
  for (int k = 0; k < 6; ++k) {
    double mn = 1e9, mx = 0, sm = 0; int n = 0;
    for (int g = 0; g < 256; ++g) { if (k >= 4 && (g & 63) >= 56) continue; double t = (double)(st[g * 64 + idx[k][0] * 8 + idx[k][1]] - t0) * 0.01; mn = t < mn ? t : mn; mx = t > mx ? t : mx; sm += t; ++n; }
    printf("DBG %s: min %.2f mean %.2f max %.2f\n", nm[k], mn, sm / n, mx);
  }
  for (int x = 1; x <= 8; ++x) {
    double mn = 1e9, mx = 0;
    for (int g = 0; g < 256; ++g) if (st[g * 64 + 7] == (u64)x) { double t = (double)(st[g * 64 + 4] - t0) * 0.01; mn = t < mn ? t : mn; mx = t > mx ? t : mx; }
    printf("DBG xcc %d Vdone %.2f..%.2f\n", x - 1, mn, mx);
  }
}

extern "C" void kernel_launch(void* const* d_in, const int* in_sizes, int n_in, void* d_out, int out_size, void* d_ws, size_t ws_size, hipStream_t stream) {
  fused_decode<<<NWG, NTHR, 0, stream>>>((const u16*)d_in[0], (const u16*)d_in[1], (const u16*)d_in[2], (const u16*)d_in[3], (const u16*)d_in[4],
                                         (const u16*)d_in[6], (const u16*)d_in[7], (u16*)d_out, (unsigned char*)d_ws);
  dbg_print<<<1, 64, 0, stream>>>((unsigned char*)d_ws);
  fprintf(stderr, "DBG ptrs x %p K %p V %p wq %p wo %p w1 %p w2 %p out %p ws %p\n", d_in[0], d_in[1], d_in[2], d_in[3], d_in[4], d_in[6], d_in[7], d_out, d_ws);
}
```

```cpp
#include <hip/hip_runtime.h>
#include <stdint.h>

typedef unsigned v4u __attribute__((ext_vector_type(4)));
typedef unsigned v2u __attribute__((ext_vector_type(2)));
typedef float f32x4 __attribute__((ext_vector_type(4)));
typedef __bf16 bf16x2 __attribute__((ext_vector_type(2)));
typedef __bf16 bf16x4 __attribute__((ext_vector_type(4)));
typedef __bf16 bf16x8 __attribute__((ext_vector_type(8)));
typedef short s16x4 __attribute__((ext_vector_type(4)));
typedef unsigned short u16;
typedef unsigned long long u64;
#define LAS __attribute__((address_space(3)))

constexpr int NB = 4, DM = 448, NH = 16, SK = 8192, DH = 256, FF = 1792;
constexpr int NWG = 256, NTHR = 512;
#define WINDOW 8
#define PRE 32
constexpr int WIN = WINDOW;
constexpr int NUNIT = 14;
constexpr int NM = 112;
constexpr int AUX_NT = 2, AUX_SC1 = 16;
#define AUX_W 2
#define CODE_PF 1
#define F_SLEEP 24
#define RU0 32
#define LATE_I 6
#define LATE_AT 15
#define LEVEL 9
#define PACE2 0
#define PACE2_STEP 8
#define PACE2_OFF 100
#define PACE 0
#define TAU8 108
#define PACE_SLEEP 8
#define PACE_DELTA 40
constexpr unsigned OOB = 0x7FFFFFF0u;
constexpr float LOG2E = 1.4426950408889634f;

constexpr unsigned WS_EPOCH = 0, WS_HB = 16384, WS_Q = 1u << 17, WS_OX = 1u << 18, WS_ML = 1u << 19, WS_Y = 3u << 18,
                   WS_O = 7u << 18, WS_BYTES = 11u << 18;
constexpr unsigned WS_STAMP = 64u << 20;
constexpr int L_WO = 0;
constexpr int L_W1 = L_WO + 28672;
constexpr int L_W2 = L_W1 + 14336;
constexpr int L_SC = L_W2 + 14336;
constexpr int L_QB = L_SC + 8192;
constexpr int L_OA = L_QB + 512;
constexpr int L_RS = L_OA + 4096;
constexpr int L_HN = L_RS + 1024;
constexpr int HN_STRIDE = 912;
constexpr int L_MISC = L_HN + 4 * HN_STRIDE;
constexpr int L_ST = L_MISC + 256;
constexpr int L_BYTES = 84 * 1024;
static_assert(L_MISC + 1024 <= L_BYTES && L_MISC % 16 == 0 && L_HN % 16 == 0, "lds");
enum { M_ML = 0, M_WGT = 8, M_CNT_ML = 12, M_CNT_HN = 13, M_RDONE = 14, M_CNT_Q = 15, M_CNT_O = 16, M_WOK = 17, M_OPUB = 18, M_TAG = 19, M_LATE = 20, M_CNT_W = 21, M_MS = 22, M_EP = 24, M_PDONE = 26, M_CNT_R = 27, M_TS = 32 };

__device__ __forceinline__ float u2f(unsigned u) { return __builtin_bit_cast(float, u); }
__device__ __forceinline__ unsigned f2u(float f) { return __builtin_bit_cast(unsigned, f); }
__device__ __forceinline__ float rbf(float f) { return (float)(__bf16)f; }
__device__ __forceinline__ unsigned pk2(float lo, float hi) { bf16x2 v = {(__bf16)lo, (__bf16)hi}; return __builtin_bit_cast(unsigned, v); }
__device__ __forceinline__ float blo(unsigned u) { return u2f(u << 16); }
__device__ __forceinline__ float bhi(unsigned u) { return u2f(u & 0xffff0000u); }
__device__ __forceinline__ float dot2(unsigned a, unsigned b, float c) {
  return __builtin_amdgcn_fdot2_f32_bf16(__builtin_bit_cast(bf16x2, a), __builtin_bit_cast(bf16x2, b), c, false);
}
template <int CTRL> __device__ __forceinline__ float dpp(float x) {
  return __builtin_bit_cast(float, __builtin_amdgcn_mov_dpp(__builtin_bit_cast(int, x), CTRL, 0xf, 0xf, true));
}
constexpr int XOR1 = 0xB1, XOR2 = 0x4E, XOR7 = 0x141, ROR8 = 0x128, ROR4 = 0x124;
__device__ __forceinline__ float swap16_add(float a, float b) {
  auto s = __builtin_amdgcn_permlane16_swap(f2u(a), f2u(b), false, false);
  const unsigned s0 = s[0], s1 = s[1];
  return u2f(s0) + u2f(s1);
}
__device__ __forceinline__ float swap32_add(float a, float b) {
  auto s = __builtin_amdgcn_permlane32_swap(f2u(a), f2u(b), false, false);
  const unsigned s0 = s[0], s1 = s[1];
  return u2f(s0) + u2f(s1);
}
__device__ __forceinline__ float swap16_max(float a) {
  auto s = __builtin_amdgcn_permlane16_swap(f2u(a), f2u(a), false, false);
  const unsigned s0 = s[0], s1 = s[1];
  return fmaxf(u2f(s0), u2f(s1));
}
__device__ __forceinline__ float swap32_max(float a) {
  auto s = __builtin_amdgcn_permlane32_swap(f2u(a), f2u(a), false, false);
  const unsigned s0 = s[0], s1 = s[1];
  return fmaxf(u2f(s0), u2f(s1));
}
__device__ __forceinline__ float row_sum(float x) { x += dpp<ROR8>(x); x += dpp<ROR4>(x); x += dpp<XOR2>(x); x += dpp<XOR1>(x); return x; }
__device__ __forceinline__ float row_max(float x) { x = fmaxf(x, dpp<ROR8>(x)); x = fmaxf(x, dpp<ROR4>(x)); x = fmaxf(x, dpp<XOR2>(x)); x = fmaxf(x, dpp<XOR1>(x)); return x; }
__device__ __forceinline__ float wave_sum(float x) { x = row_sum(x); x = swap16_add(x, x); return swap32_add(x, x); }
__device__ __forceinline__ float wave_max(float x) { x = row_max(x); x = swap16_max(x); return swap32_max(x); }

__device__ __forceinline__ __amdgpu_buffer_rsrc_t mkrs(const void* p, unsigned bytes) { return __builtin_amdgcn_make_buffer_rsrc((void*)p, 0, bytes, 0x00020000); }
#define LD128(rs, vo, so, aux) __builtin_amdgcn_raw_buffer_load_b128(rs, vo, so, aux)
#define LD64(rs, vo, so, aux) __builtin_amdgcn_raw_buffer_load_b64(rs, vo, so, aux)
#define ST128(v, rs, vo, so, aux) __builtin_amdgcn_raw_buffer_store_b128(v, rs, vo, so, aux)
#define ST64(v, rs, vo, so, aux) __builtin_amdgcn_raw_buffer_store_b64(v, rs, vo, so, aux)
#define ST32(v, rs, vo, so, aux) __builtin_amdgcn_raw_buffer_store_b32(v, rs, vo, so, aux)
#define PIN() asm volatile("" ::: "memory")
#define USE(x) asm volatile("" :: "v"(x) : "memory")
#define REDEF(x) asm volatile("" : "+v"(x) :: "memory")
#define BARRIER() asm volatile("s_waitcnt lgkmcnt(0)\n\ts_barrier" ::: "memory")
#define DRAIN() asm volatile("s_waitcnt vmcnt(0)" ::: "memory")
#define LGKM0() asm volatile("s_waitcnt lgkmcnt(0)" ::: "memory")
constexpr int SPIN_CAP = 1 << 16;
constexpr int LSPIN_CAP = 1 << 20;

#define STAMP(i) do {} while (0)
#define RET() return

__device__ __forceinline__ void ln8(float (&v)[8], int lane) {
  float s = ((v[0] + v[1]) + (v[2] + v[3])) + ((v[4] + v[5]) + (v[6] + v[7]));
  const float mean = wave_sum(s) * (1.0f / DM);
  float q = 0.f;
#pragma unroll
  for (int e = 0; e < 8; ++e) { const float d = v[e] - mean; q = fmaf(d, d, q); }
  q = lane < 56 ? q : 0.f;
  const float var = wave_sum(q) * (1.0f / DM);
  const float mb = rbf(mean);
  const float den = rbf(__builtin_amdgcn_sqrtf(rbf(rbf(var) + 1e-5f)));
  const float inv = __builtin_amdgcn_rcpf(den);
#pragma unroll
  for (int e = 0; e < 8; ++e) v[e] = rbf(v[e] - mb) * inv;
}
__device__ __forceinline__ float reduce_pieces(f32x4 v) {
  const float a = swap32_add(v[0], v[2]), b = swap32_add(v[1], v[3]);
  float c = swap16_add(a, b);
  c += dpp<ROR8>(c); c += dpp<ROR4>(c); c += dpp<XOR2>(c);
  return c;
}
__device__ __forceinline__ v4u enc4(f32x4 v, unsigned tag) {
  v4u r;
  r[0] = __builtin_amdgcn_perm(f2u(v[0]), tag, 0x07060500u); r[1] = __builtin_amdgcn_perm(f2u(v[1]), tag, 0x07060501u);
  r[2] = __builtin_amdgcn_perm(f2u(v[2]), tag, 0x07060502u); r[3] = __builtin_amdgcn_perm(f2u(v[3]), tag, 0x07060503u);
  return r;
}
__device__ __forceinline__ bool valid4(v4u a, unsigned tag) {
  return (__builtin_amdgcn_perm(a[1], a[0], 0x0c0c0400u) | __builtin_amdgcn_perm(a[3], a[2], 0x04000c0cu)) == tag;
}
__device__ __forceinline__ f32x4 dec4(v4u a) { return __builtin_bit_cast(f32x4, (a & 0xFFFFFF00u) | 0x80u); }
__device__ __forceinline__ f32x4 dec4x(v4u a) { return __builtin_bit_cast(f32x4, a & 0xFFFFFF00u); }

#define TS(i) do {} while (0)
#define SPIN(cap) _Pragma("unroll 1") for (int spin = 0; spin < (cap); ++spin)
#define LDS_INC(w) __hip_atomic_fetch_add((LAS unsigned*)(smem + L_MISC) + (w), 1u, __ATOMIC_RELAXED, __HIP_MEMORY_SCOPE_WORKGROUP)
#define LDS_WAIT_GE(w, n, slp) do { SPIN(LSPIN_CAP) { PIN(); if (misc[w] >= (unsigned)(n)) break; if (slp) __builtin_amdgcn_s_sleep(slp); } PIN(); } while (0)
template <int CTRL> __device__ __forceinline__ float comb(bool bit, float lo, float hi) {
  const float a = lo + dpp<CTRL>(lo), b = hi + dpp<CTRL>(hi);
  return bit ? b : a;
}
__device__ __forceinline__ f32x4 sel4(bool s1, bool s2, f32x4 a, f32x4 b, f32x4 c, f32x4 d) {
  f32x4 r;
#pragma unroll
  for (int k = 0; k < 4; ++k) { const float lo = s1 ? b[k] : a[k], hi = s1 ? d[k] : c[k]; r[k] = s2 ? hi : lo; }
  return r;
}
__device__ __forceinline__ bf16x4 trd(const LAS unsigned char* p) { return __builtin_amdgcn_ds_read_tr16_b64_v4bf16((LAS bf16x4*)p); }
__device__ __forceinline__ unsigned now32() { return (unsigned)__builtin_amdgcn_s_memrealtime(); }

__global__ void __launch_bounds__(NTHR, 2)
fused_decode(const u16* x, const u16* Kc, const u16* Vc, const u16* wq, const u16* wo, const u16* w1, const u16* w2, u16* out, unsigned char* ws) {
  __shared__ __attribute__((aligned(16))) unsigned char smem[L_BYTES];
  const int tid = threadIdx.x, lane = tid & 63;
  int wave = __builtin_amdgcn_readfirstlane(tid >> 6);
  const int g = blockIdx.x;
  const int b = g >> 6, h = (g >> 2) & 15, sp = g & 3;
  const int half = lane >> 5, c32 = lane & 31, g4 = lane >> 4, c16 = lane & 15;
  volatile LAS unsigned* misc = (volatile LAS unsigned*)(smem + L_MISC);
  const __amdgpu_buffer_rsrc_t rws = mkrs(ws, WS_BYTES);
  const __amdgpu_buffer_rsrc_t rk = mkrs(Kc + (size_t)g * (2048 * DH), 2048 * DH * 2);
  const __amdgpu_buffer_rsrc_t rx = mkrs(x, NB * DM * 2);
  const int voff = tid * 16;
  const int hw = wave - 4;
  v4u kb[PRE];
  u64 pc0;
  asm volatile("s_getpc_b64 %0\n.Lfd_code_begin:" : "=s"(pc0));
  unsigned clen, clen2;
  asm volatile("s_mov_b32 %0, .Lfd_code_end-.Lfd_code_begin\n\ts_mov_b32 %1, .Lfd_code_end2-.Lfd_code_begin" : "=s"(clen), "=s"(clen2));
  clen = clen > clen2 ? clen : clen2;
  const unsigned tstart = now32() + PACE2_OFF;
  if (wave < 4) {
#pragma unroll
    for (int i = 0; i < WIN; ++i) kb[i] = LD128(rk, voff, i * 4096, AUX_NT);
#pragma unroll
    for (int i = WIN; i < 12; ++i) asm volatile("" : "=v"(kb[i]));
  } else {
    const __amdgpu_buffer_rsrc_t rq = mkrs(wq + h * DH + (sp * 4 + b) * 16, LEVEL < 1 ? 0 : (DM - 1) * (NH * DH * 2) + 32);
    kb[0] = LD128(rx, lane < 56 ? lane * 16 : (int)OOB, hw * (DM * 2), 0);
#pragma unroll
    for (int i = 0; i < 4; ++i) kb[1 + i] = LD128(rq, (lane >> 1) * (NH * DH * 2) + (lane & 1) * 16, (hw + 4 * i) * 32 * (NH * DH * 2), AUX_W);
    kb[5] = LD128(rws, 0, WS_EPOCH, 0);
    {
      const __amdgpu_buffer_rsrc_t rc = mkrs((const void*)pc0, clen);
#pragma unroll
      for (int i = 0; i < 6; ++i) kb[6 + i] = LD128(rc, voff - 4096, i * 4096, 0);
    }
#pragma unroll
    for (int i = 12; i < WIN; ++i) asm volatile("" : "=v"(kb[i]));
    if (wave == 4 && lane >= 12 && lane < 32) misc[lane] = 0u;
  }
  BARRIER();
  asm volatile("" : "+s"(wave));

  const int unit = g & 63;
  const int oct = (g >> 7) * 16 + h, pj = (b & 1) * 4 + sp;
  if (wave < 4) {
    const __amdgpu_buffer_rsrc_t rv = mkrs(Vc + (size_t)g * (2048 * DH), 2048 * DH * 2);
#pragma unroll
    for (int i = WIN; i < PRE; ++i) { USE(kb[i - WIN]); kb[i] = LD128(rk, voff, i * 4096, AUX_NT); PIN(); }
    STAMP(1);
    BARRIER();
    STAMP(2);
    const v4u qv = *(const v4u*)(smem + L_QB + c32 * 16);
    const unsigned tag = __builtin_amdgcn_readfirstlane(misc[M_TAG]);
    float* sc = (float*)(smem + L_SC) + (wave * 2 + half) * 256;
    float* scw = sc + (((c32 >> 4) & 1) | ((c32 >> 2) & 2) | (c32 & 4) | ((c32 << 2) & 8));
    const bool b3 = c32 & 8, b2 = c32 & 4, b1 = c32 & 2;
    float m = -INFINITY;
    float t0_, t1_, t2_, t3_;
#define COMB(CTRL, BIT, lo, hi) comb<CTRL>(BIT, lo, hi)
#define KSTEP(v, i, grp) do { \
      float d_ = dot2((v)[0], qv[0], 0.f); d_ = dot2((v)[1], qv[1], d_); d_ = dot2((v)[2], qv[2], d_); d_ = dot2((v)[3], qv[3], d_); \
      if (((i) & 1) == 0) t0_ = d_; else { d_ = swap16_add(t0_, d_); \
        if (((i) & 2) == 0) t1_ = d_; else { d_ = COMB(ROR8, b3, t1_, d_); \
          if (((i) & 4) == 0) t2_ = d_; else { d_ = COMB(XOR7, b2, t2_, d_); \
            if (((i) & 8) == 0) t3_ = d_; else { d_ = COMB(XOR2, b1, t3_, d_); d_ += dpp<XOR1>(d_); \
              d_ = rbf(d_) * 0.0625f; m = fmaxf(m, d_); scw[(grp) * 16] = d_; } } } } \
      asm volatile("" : "+v"(t0_), "+v"(t1_), "+v"(t2_), "+v"(t3_), "+v"(m)); } while (0)
#define PACE2_AT(row) do {} while (0)
    asm volatile("" : "=v"(t0_), "=v"(t1_), "=v"(t2_), "=v"(t3_));
#pragma unroll
    for (int i = 0; i < PRE; ++i) {
      REDEF(kb[i]);
      KSTEP(kb[i], i & 15, i >> 4);
      if (i < WIN) { PIN(); kb[i] = LD128(rk, voff, (PRE + i) * 4096, AUX_NT); PIN(); }
    }
    TS(0);
#pragma unroll 1
    for (int gi = PRE / 16; gi < 16; ++gi) {
      const __amdgpu_buffer_rsrc_t rn = gi < 15 ? rk : rv;
      const int offn = gi < 15 ? (gi + 1) * 16 * 4096 : 0;
#pragma unroll
      for (int i = 0; i < 16; ++i) {
        if ((i & (PACE2_STEP - 1)) == 0) PACE2_AT(gi * 16 + i);
        USE(kb[i]);
        if (i + WIN < 16) kb[i + WIN] = LD128(rk, voff, (gi * 16 + i + WIN) * 4096, AUX_NT);
        else kb[i + WIN - 16] = LD128(rn, voff, offn + (i + WIN - 16) * 4096, AUX_NT);
        REDEF(kb[i]);
        KSTEP(kb[i], i, gi);
      }
      if (gi & 1) TS(gi >> 1);
    }
    STAMP(3);
    m = wave_max(m);
    {
      LGKM0();
      f32x4* ps = (f32x4*)(smem + L_SC) + wave * 128 + lane * 2;
      f32x4 a = ps[0], bq = ps[1];
      const float mm = m * LOG2E;
#pragma unroll
      for (int e = 0; e < 4; ++e) { a[e] = __builtin_amdgcn_exp2f(fmaf(a[e], LOG2E, -mm)); bq[e] = __builtin_amdgcn_exp2f(fmaf(bq[e], LOG2E, -mm)); }
      ps[0] = a; ps[1] = bq;
      float l = ((a[0] + a[1]) + (a[2] + a[3])) + ((bq[0] + bq[1]) + (bq[2] + bq[3]));
      l = wave_sum(l);
      if (lane == 0) { misc[M_ML + 2 * wave] = f2u(m); misc[M_ML + 2 * wave + 1] = f2u(l); }
      LGKM0();
      if (lane == 0) LDS_INC(M_CNT_ML);
    }
    float acc[8], wgt = 0.f;
#pragma unroll
    for (int k = 0; k < 8; ++k) acc[k] = 0.f;
#define VBODY(gi, LAST) do { \
      f32x4 p4[4]; \
      _Pragma("unroll") for (int i = 0; i < 4; ++i) p4[i] = *(const f32x4*)(sc + (gi) * 16 + i * 4); \
      TS(8 + (gi)); \
      _Pragma("unroll") for (int i = 0; i < 16; ++i) { \
        if ((i & (PACE2_STEP - 1)) == 0) PACE2_AT(256 + (gi) * 16 + i); \
        if ((LAST) && i == LATE_I && tid == 0) misc[M_LATE] = 1u; \
        USE(kb[i]); \
        if (!(LAST) || i + WIN < 16) kb[(i + WIN) & 15] = LD128(rv, voff, ((gi) * 16 + i + WIN) * 4096, AUX_NT); \
        REDEF(kb[i]); \
        const float p = p4[i >> 2][i & 3]; \
        _Pragma("unroll") for (int k = 0; k < 4; ++k) { acc[2 * k] = fmaf(p, blo(kb[i][k]), acc[2 * k]); acc[2 * k + 1] = fmaf(p, bhi(kb[i][k]), acc[2 * k + 1]); } \
        asm volatile("" : "+v"(acc[0]), "+v"(acc[1]), "+v"(acc[2]), "+v"(acc[3]), "+v"(acc[4]), "+v"(acc[5]), "+v"(acc[6]), "+v"(acc[7])); \
      } } while (0)
#pragma unroll 1
    for (int gi = 0; gi < 15; ++gi) VBODY(gi, false);
    LDS_WAIT_GE(M_WOK, 1, 2);
    wgt = u2f(misc[M_WGT + wave]);
    VBODY(15, true);
    STAMP(4);
    {
      f32x4 o;
#pragma unroll
      for (int k = 0; k < 4; ++k) o[k] = swap32_add(acc[k], acc[k + 4]) * wgt;
      f32x4* oa = (f32x4*)(smem + L_OA) + lane;
      oa[wave * 64] = o;
      LGKM0();
      unsigned old = 0;
      if (lane == 0) old = LDS_INC(M_CNT_O);
      if (__builtin_amdgcn_readfirstlane(old) == 3u) {
        PIN();
        o = (oa[0] + oa[64]) + (oa[128] + oa[192]);
        ST128(enc4(o, tag), rws, (c32 >> 2) * 1024 + (c32 & 3) * 32 + half * 16, WS_OX + oct * 8192 + pj * 128, AUX_SC1);
      }
    }
    STAMP(5);
    const int ru = unit - RU0;
    if (ru < 0 || ru >= NUNIT) RET();
    const v2u xr = LD64(rx, (lane & 7) * 8, b * (DM * 2) + ru * 64, 0);
    const int po = (lane >> 3) * (DM * 4) + (lane & 7) * 16;
    float* rs = (float*)(smem + L_RS);
    LDS_WAIT_GE(M_PDONE, 1, 1);
#pragma unroll 1
    for (int ph = 0; ph < 2; ++ph) {
      const unsigned ib = (ph == 0 ? WS_Y + (unsigned)(b * 128 + wave * 32) * (DM * 4u) : WS_O + (unsigned)(b * NM + wave * 28) * (DM * 4u)) + ru * 128;
      v4u p0 = {0u, 0u, 0u, 0u}, p1 = p0, p2 = p0, p3 = p0;
      bool k0 = false, k1 = false, k2 = false, k3 = ph == 1 && lane >= 32;
      SPIN(SPIN_CAP) {
        PIN();
        const v4u n0 = LD128(rws, k0 ? (int)OOB : po, ib, AUX_SC1), n1 = LD128(rws, k1 ? (int)OOB : po, ib + 8 * DM * 4, AUX_SC1);
        const v4u n2 = LD128(rws, k2 ? (int)OOB : po, ib + 16 * DM * 4, AUX_SC1), n3 = LD128(rws, k3 ? (int)OOB : po, ib + 24 * DM * 4, AUX_SC1);
        p0 = k0 ? p0 : n0; p1 = k1 ? p1 : n1; p2 = k2 ? p2 : n2; p3 = k3 ? p3 : n3;
        k0 = valid4(p0, tag); k1 = valid4(p1, tag); k2 = valid4(p2, tag); k3 |= valid4(p3, tag);
        if (__all(k0 & k1 & k2 & k3)) break;
      }
      if (ph == 0 || (wave & 1)) STAMP(6 + ph);
      const f32x4 v = (dec4(p0) + dec4(p1)) + (dec4(p2) + dec4(p3));
      float c = swap16_add(swap32_add(v[0], v[2]), swap32_add(v[1], v[3]));
      c += dpp<ROR8>(c);
      rs[wave * 32 + (lane & 7) * 4 + 2 * half + (g4 & 1)] = c;
      LGKM0();
      unsigned old = 0;
      if (lane == 0) old = LDS_INC(M_CNT_R);
      if (__builtin_amdgcn_readfirstlane(old) == 3u + 4u * ph) {
        PIN();
        const f32x4* r4 = (const f32x4*)rs + (lane & 7);
        const f32x4 sum = (r4[0] + r4[8]) + (r4[16] + r4[24]);
        f32x4 res;
        if (ph == 0) {
          res[0] = rbf(blo(xr[0]) + rbf(sum[0])); res[1] = rbf(bhi(xr[0]) + rbf(sum[1])); res[2] = rbf(blo(xr[1]) + rbf(sum[2])); res[3] = rbf(bhi(xr[1]) + rbf(sum[3]));
          ST128(enc4(res, tag), rws, (lane >> 3) * (NB * DM * 4) + (lane & 7) * 16, WS_HB + b * (DM * 4) + ru * 128, AUX_SC1);
          *((f32x4*)rs + 32 + (lane & 7)) = res;
          LGKM0();
          if (lane == 0) misc[M_RDONE] = 1u;
        } else {
          const f32x4 hq = r4[32];
          const v2u ov = {pk2(hq[0] + rbf(sum[0]), hq[1] + rbf(sum[1])), pk2(hq[2] + rbf(sum[2]), hq[3] + rbf(sum[3]))};
          ST64(ov, mkrs(out, NB * DM * 2), lane < 8 ? lane * 8 : (int)OOB, b * (DM * 2) + ru * 64, 0);
          if (g == RU0) {
            const v2u ne = {misc[M_EP], misc[M_EP + 1]};
            ST64(ne, rws, lane == 0 ? 0 : (int)OOB, WS_EPOCH, AUX_SC1);
          }
        }
      }
      if (ph == 0) LDS_WAIT_GE(M_RDONE, 1, 1);
    }
    STAMP(1);
    asm volatile(".Lfd_code_end2:");
    RET();
  }
  const bool is_m = unit < 28;
  const int mi = unit * 4 + b;
  const int tq = c16 >> 2, tp = c16 & 3;
  const LAS unsigned char* s1 = (const LAS unsigned char*)(smem + L_W1) + (g4 * 8 + tq) * 32 + tp * 8;
  const unsigned char* hp = smem + L_HN + (c16 & 3) * HN_STRIDE + g4 * 16;
  {
    float v[8];
#pragma unroll
    for (int k = 0; k < 4; ++k) { v[2 * k] = blo(kb[0][k]); v[2 * k + 1] = bhi(kb[0][k]); }
    ln8(v, lane);
    const v4u xn = {pk2(v[0], v[1]), pk2(v[2], v[3]), pk2(v[4], v[5]), pk2(v[6], v[7])};
    if (lane < 56) *(v4u*)(smem + L_HN + hw * HN_STRIDE + lane * 16) = xn;
#pragma unroll
    for (int i = 0; i < 4; ++i) if (hw + 4 * i < 14) *(v4u*)(smem + L_W1 + (hw + 4 * i) * 1024 + lane * 16) = kb[1 + i];
    LGKM0();
    if (lane == 0) LDS_INC(M_CNT_Q);
  }
  const unsigned e0 = __builtin_amdgcn_readfirstlane(kb[5][0]), e1 = __builtin_amdgcn_readfirstlane(kb[5][1]);
  unsigned qtag = e0 + 0x9E3779B9u;
  qtag ^= qtag >> 16; qtag *= 0x85EBCA6Bu; qtag ^= qtag >> 13; qtag *= 0xC2B2AE35u; qtag ^= qtag >> 16;
  if (hw == 0) {
    LDS_WAIT_GE(M_CNT_Q, 4, 0);
    bf16x8 a1[14], bfr[14];
#pragma unroll
    for (int s = 0; s < 14; ++s) {
      a1[s] = __builtin_shufflevector(trd(s1 + s * 1024), trd(s1 + s * 1024 + 128), 0, 1, 2, 3, 4, 5, 6, 7);
      bfr[s] = *(const bf16x8*)(hp + s * 64);
    }
    __builtin_amdgcn_sched_barrier(0);
    f32x4 da = {0.f, 0.f, 0.f, 0.f}, db = {0.f, 0.f, 0.f, 0.f};
#pragma unroll
    for (int s = 0; s < 14; s += 2) {
      da = __builtin_amdgcn_mfma_f32_16x16x32_bf16(a1[s], bfr[s], da, 0, 0, 0);
      db = __builtin_amdgcn_mfma_f32_16x16x32_bf16(a1[s + 1], bfr[s + 1], db, 0, 0, 0);
    }
    da += db;
    const v4u qg = {pk2(da[0], da[1]), qtag, pk2(da[2], da[3]), qtag};
    ST128(qg, rws, c16 < 4 ? c16 * 64 + g4 * 16 : (int)OOB, WS_Q + h * 4096 + (sp * 4 + b) * 256, AUX_SC1);
    if (lane == 0) LDS_INC(M_CNT_Q);
    STAMP(1);
  } else if (hw == 3) {
    if (lane == 0) { misc[M_TAG] = qtag; misc[M_EP] = e0 + 1u; misc[M_EP + 1] = e1; }
  } else if (hw == 1) {
    v4u qg;
    LDS_WAIT_GE(M_CNT_Q, 5, 1);
    SPIN(SPIN_CAP) {
      PIN();
      qg = LD128(rws, (lane >> 2) * 256 + (lane & 3) * 16, WS_Q + h * 4096 + b * 64, AUX_SC1);
      if (__all(qg[1] == qtag && qg[3] == qtag)) break;
    }
    *(v2u*)(smem + L_QB + lane * 8) = v2u{qg[0], qg[2]};
    STAMP(1);
  }
#pragma unroll
  for (int i = 6; i < 12; ++i) USE(kb[i]);
  BARRIER();
  bf16x8 a0[8];
  {
    const __amdgpu_buffer_rsrc_t r0 = mkrs(wo + (size_t)(h * DH + pj * 32) * DM, 32 * DM * 2);
    v4u t0[7];
#pragma unroll
    for (int i = 0; i < 7; ++i) t0[i] = LD128(r0, lane * 16, (hw + 4 * i) * 1024, AUX_W);
    const unsigned d1 = __builtin_amdgcn_raw_buffer_load_b16(mkrs(hw == 0 ? w1 : hw == 1 ? w2 : hw == 2 ? w1 + DM * FF - 64 : w2 + DM * FF - 64, 64), 0, 0, 0);
#pragma unroll
    for (int i = 0; i < 7; ++i) *(v4u*)(smem + L_WO + (hw + 4 * i) * 1024 + lane * 16) = t0[i];
    USE(d1);
    LGKM0();
    if (lane == 0) LDS_INC(M_CNT_W);
    LDS_WAIT_GE(M_CNT_W, 4, 2);
    const LAS unsigned char* s0 = (const LAS unsigned char*)(smem + L_WO) + (g4 * 8 + tq) * (DM * 2) + (hw * 64 + tp * 4) * 2;
#pragma unroll
    for (int t = 0; t < 8; ++t) a0[t] = __builtin_shufflevector(trd(s0 + (t & 3) * 32 + (t >> 2) * 512), trd(s0 + (t & 3) * 32 + (t >> 2) * 512 + 4 * DM * 2), 0, 1, 2, 3, 4, 5, 6, 7);
#pragma unroll
    for (int t = 0; t < 8; ++t) asm volatile("" : "+v"(a0[t]));
  }
  if (hw == 0) {
    LDS_WAIT_GE(M_CNT_ML, 4, 16);
    const float mo = u2f(misc[M_ML + 2 * (lane & 3)]), lo = u2f(misc[M_ML + 2 * (lane & 3) + 1]);
    float M = fmaxf(mo, dpp<XOR1>(mo)); M = fmaxf(M, dpp<XOR2>(M));
    const float e = __builtin_amdgcn_exp2f((mo - M) * LOG2E);
    float L = lo * e; L += dpp<XOR1>(L); L += dpp<XOR2>(L);
    if (lane < 4) misc[M_WGT + lane] = f2u(e);
    if (lane == 0) { misc[M_MS] = f2u(M); misc[M_MS + 1] = f2u(L); }
    LGKM0();
    if (lane == 0) misc[M_WOK] = 1u;
    const unsigned dummy = __builtin_amdgcn_raw_buffer_load_b16(mkrs(out, NB * DM * 2), 0, 0, 0);
    USE(dummy);
  }
  STAMP(4);
  LDS_WAIT_GE(M_LATE, 1, 4);
  if (hw == 0) {
    const f32x4 mlv = {u2f(misc[M_MS]), u2f(misc[M_MS + 1]), 0.f, 0.f};
    ST128(enc4(mlv, qtag), rws, lane < 8 ? lane * 1024 : (int)OOB, WS_ML + oct * 8192 + pj * 128, AUX_SC1);
  }
  v4u t1[4], t2[4];
  {
    const __amdgpu_buffer_rsrc_t r1 = mkrs(w1 + mi * 16, is_m ? (DM - 1) * (FF * 2) + 32 : 0);
    const __amdgpu_buffer_rsrc_t r2 = mkrs(w2 + (size_t)mi * 16 * DM, is_m ? 16 * DM * 2 : 0);
#pragma unroll
    for (int i = 0; i < 4; ++i) {
      t1[i] = LD128(r1, (lane >> 1) * (FF * 2) + (lane & 1) * 16, (hw + 4 * i) * 32 * (FF * 2), AUX_W);
      t2[i] = LD128(r2, lane * 16, (hw + 4 * i) * 1024, AUX_W);
    }
    PIN();
  }
  STAMP(3);
  {
    v4u pv;
    SPIN(SPIN_CAP) {
      PIN();
      pv = LD128(rws, (c16 & 7) * 128, WS_ML + oct * 8192 + pj * 1024, AUX_SC1);
      if (__all(valid4(pv, qtag))) break;
      __builtin_amdgcn_s_sleep(1);
    }
    float wn;
    {
      const float ms = u2f(pv[0] & 0xFFFFFF00u), ls = u2f((pv[1] & 0xFFFFFF00u) | 0x80u);
      float M = fmaxf(ms, dpp<XOR1>(ms)); M = fmaxf(M, dpp<XOR2>(M));
      const float e = __builtin_amdgcn_exp2f((ms - M) * LOG2E);
      float L = ls * e; L += dpp<XOR1>(L); L += dpp<XOR2>(L);
      wn = e * __builtin_amdgcn_rcpf(L);
    }
    const int po = ((c16 >> 2) & 1) * 512 + (c16 & 3) * 128 + g4 * 32 + (c16 >> 3) * 16;
    SPIN(SPIN_CAP) {
      PIN();
      pv = LD128(rws, po, WS_OX + oct * 8192 + pj * 1024, AUX_SC1);
      if (__all(valid4(pv, qtag))) break;
    }
    STAMP(5);
    f32x4 o = dec4(pv);
#pragma unroll
    for (int k = 0; k < 4; ++k) { o[k] *= wn; o[k] += dpp<XOR1>(o[k]); o[k] += dpp<XOR2>(o[k]); }
    v4u bo;
    bo[0] = pk2(o[0], o[1]); bo[1] = pk2(o[2], o[3]);
    bo[2] = f2u(dpp<ROR8>(u2f(bo[0]))); bo[3] = f2u(dpp<ROR8>(u2f(bo[1])));
    f32x4 y[8];
#pragma unroll
    for (int t = 0; t < 8; ++t) y[t] = __builtin_amdgcn_mfma_f32_16x16x32_bf16(a0[t], __builtin_bit_cast(bf16x8, bo), f32x4{0.f, 0.f, 0.f, 0.f}, 0, 0, 0);
    __builtin_amdgcn_sched_barrier(0);
    const int vo = c16 < 8 ? (c16 >> 2) * (128 * DM * 4) + (c16 & 3) * 64 + g4 * 16 : (int)OOB;
    const bool s1b = c16 & 1, s2b = c16 & 2;
    const unsigned yb = WS_Y + (unsigned)((b & 2) * 128 + h * 8 + pj) * (DM * 4u) + hw * 256;
    ST128(enc4(sel4(s1b, s2b, y[0], y[1], y[2], y[3]), qtag), rws, vo, yb, AUX_SC1);
    ST128(enc4(sel4(s1b, s2b, y[4], y[5], y[6], y[7]), qtag), rws, hw < 3 ? vo : (int)OOB, yb + 1024, AUX_SC1);
    if (lane == 0) misc[M_PDONE] = 1u;
  }
  STAMP(6);
  if (!is_m) RET();
  bf16x8 a1[14];
  s16x4 a2[8];
  {
#pragma unroll
    for (int i = 0; i < 4; ++i) if (hw + 4 * i < 14) {
      *(v4u*)(smem + L_W1 + (hw + 4 * i) * 1024 + lane * 16) = t1[i];
      *(v4u*)(smem + L_W2 + (hw + 4 * i) * 1024 + lane * 16) = t2[i];
    }
    LGKM0();
    if (lane == 0) LDS_INC(M_CNT_W);
    LDS_WAIT_GE(M_CNT_W, 8, 0);
    const LAS unsigned char* s2 = (const LAS unsigned char*)(smem + L_W2) + (g4 * 4 + tq) * (DM * 2) + (hw * 64 + tp * 4) * 2;
#pragma unroll
    for (int t = 0; t < 8; ++t) a2[t] = __builtin_bit_cast(s16x4, trd(s2 + (t & 3) * 32 + (t >> 2) * 512));
    asm volatile("" : "+v"(a2[7]));
#pragma unroll
    for (int s = 0; s < 14; ++s) a1[s] = __builtin_shufflevector(trd(s1 + s * 1024), trd(s1 + s * 1024 + 128), 0, 1, 2, 3, 4, 5, 6, 7);
#pragma unroll
    for (int t = 0; t < 7; ++t) asm volatile("" : "+v"(a2[t]), "+v"(a1[2 * t]), "+v"(a1[2 * t + 1]));
  }
  v4u h0, h1;
  {
    const unsigned hb = WS_HB + (unsigned)(g & 7) * (NB * DM * 4) + hw * (DM * 4);
    const int vo = lane < 56 ? lane * 32 : (int)OOB;
    SPIN(SPIN_CAP) {
      PIN();
      h0 = LD128(rws, vo, hb, AUX_SC1); h1 = LD128(rws, vo, hb + 16, AUX_SC1);
      const bool ok = (valid4(h0, qtag) & valid4(h1, qtag)) | (lane >= 56);
      if (__all(ok)) break;
    }
  }
  STAMP(7);
  {
    float v[8];
#pragma unroll
    for (int k = 0; k < 4; ++k) { v[k] = bhi(h0[k]); v[k + 4] = bhi(h1[k]); }
    ln8(v, lane);
    const v4u hn = {pk2(v[0], v[1]), pk2(v[2], v[3]), pk2(v[4], v[5]), pk2(v[6], v[7])};
    if (lane < 56) *(v4u*)(smem + L_HN + hw * HN_STRIDE + lane * 16) = hn;
    LGKM0();
    if (lane == 0) LDS_INC(M_CNT_HN);
    LDS_WAIT_GE(M_CNT_HN, 4, 0);
  }
  f32x4 d1a = {0.f, 0.f, 0.f, 0.f}, d1b = {0.f, 0.f, 0.f, 0.f};
  {
    bf16x8 bfr[14];
#pragma unroll
    for (int s = 0; s < 14; ++s) bfr[s] = *(const bf16x8*)(hp + s * 64);
    __builtin_amdgcn_sched_barrier(0);
#pragma unroll
    for (int s = 0; s < 14; s += 2) {
      d1a = __builtin_amdgcn_mfma_f32_16x16x32_bf16(a1[s], bfr[s], d1a, 0, 0, 0);
      d1b = __builtin_amdgcn_mfma_f32_16x16x32_bf16(a1[s + 1], bfr[s + 1], d1b, 0, 0, 0);
    }
  }
  float sv[4];
#pragma unroll
  for (int r = 0; r < 4; ++r) { const float t = rbf(d1a[r] + d1b[r]); sv[r] = t * __builtin_amdgcn_rcpf(1.0f + __builtin_amdgcn_exp2f(-t * LOG2E)); }
  const v2u hbp = {pk2(sv[0], sv[1]), pk2(sv[2], sv[3])};
  const s16x4 b2v = __builtin_bit_cast(s16x4, hbp);
  const int vo = (c16 & 3) * (NM * DM * 4) + (c16 >> 2) * 64 + g4 * 16;
  f32x4 o2[8];
#pragma unroll
  for (int t = 0; t < 8; ++t) o2[t] = __builtin_amdgcn_mfma_f32_16x16x16bf16_1k(a2[t], b2v, f32x4{0.f, 0.f, 0.f, 0.f}, 0, 0, 0);
  __builtin_amdgcn_sched_barrier(0);
  {
    const bool s1b = c16 & 4, s2b = c16 & 8;
    const unsigned ob = WS_O + (unsigned)mi * (DM * 4u) + hw * 256;
    ST128(enc4(sel4(s1b, s2b, o2[0], o2[1], o2[2], o2[3]), qtag), rws, vo, ob, AUX_SC1);
    ST128(enc4(sel4(s1b, s2b, o2[4], o2[5], o2[6], o2[7]), qtag), rws, hw < 3 ? vo : (int)OOB, ob + 1024, AUX_SC1);
  }
  STAMP(2);
  asm volatile(".Lfd_code_end:");
  RET();
}

extern "C" void kernel_launch(void* const* d_in, const int* in_sizes, int n_in, void* d_out, int out_size, void* d_ws, size_t ws_size, hipStream_t stream) {
  fused_decode<<<NWG, NTHR, 0, stream>>>((const u16*)d_in[0], (const u16*)d_in[1], (const u16*)d_in[2], (const u16*)d_in[3], (const u16*)d_in[4],
                                         (const u16*)d_in[6], (const u16*)d_in[7], (u16*)d_out, (unsigned char*)d_ws);
}
```

```cpp
#include <hip/hip_runtime.h>
#include <stdint.h>

typedef unsigned v4u __attribute__((ext_vector_type(4)));
typedef unsigned v2u __attribute__((ext_vector_type(2)));
typedef float f32x4 __attribute__((ext_vector_type(4)));
typedef __bf16 bf16x2 __attribute__((ext_vector_type(2)));
typedef __bf16 bf16x4 __attribute__((ext_vector_type(4)));
typedef __bf16 bf16x8 __attribute__((ext_vector_type(8)));
typedef short s16x4 __attribute__((ext_vector_type(4)));
typedef unsigned short u16;
typedef unsigned long long u64;
#define LAS __attribute__((address_space(3)))

constexpr int NB = 4, DM = 448, NH = 16, SK = 8192, DH = 256, FF = 1792;
constexpr int NWG = 256, NTHR = 512;
#define WINDOW 8
#define PRE 32
constexpr int WIN = WINDOW;
constexpr int NUNIT = 14;
constexpr int NM = 112;
constexpr int AUX_NT = 2, AUX_SC1 = 16;
#define AUX_K 2
#define VTOUCH 0
#define AUX_W 0
#define CODE_PF 1
#define F_SLEEP 24
#define RU0 32
#define LATE_I 6
#define LATE_AT 15
#define LEVEL 9
#define PACE2 0
#define PACE2_STEP 8
#define PACE2_OFF 100
#define PACE 0
#define TAU8 108
#define PACE_SLEEP 8
#define PACE_DELTA 40
constexpr unsigned OOB = 0x7FFFFFF0u;
constexpr float LOG2E = 1.4426950408889634f;

constexpr unsigned WS_EPOCH = 0, WS_HB = 16384, WS_Q = 1u << 17, WS_OX = 1u << 18, WS_ML = 1u << 19, WS_Y = 3u << 18,
                   WS_O = 7u << 18, WS_BYTES = 11u << 18;
constexpr unsigned WS_STAMP = 64u << 20;
constexpr int L_WO = 0;
constexpr int L_W1 = L_WO + 28672;
constexpr int L_W2 = L_W1 + 14336;
constexpr int L_SC = L_W2 + 14336;
constexpr int L_QB = L_SC + 8192;
constexpr int L_OA = L_QB + 512;
constexpr int L_RS = L_OA + 4096;
constexpr int L_HN = L_RS + 1024;
constexpr int HN_STRIDE = 912;
constexpr int L_MISC = L_HN + 4 * HN_STRIDE;
constexpr int L_ST = L_MISC + 256;
constexpr int L_BYTES = 84 * 1024;
static_assert(L_MISC + 1024 <= L_BYTES && L_MISC % 16 == 0 && L_HN % 16 == 0, "lds");
enum { M_ML = 0, M_WGT = 8, M_CNT_ML = 12, M_CNT_HN = 13, M_RDONE = 14, M_CNT_Q = 15, M_CNT_O = 16, M_WOK = 17, M_OPUB = 18, M_TAG = 19, M_LATE = 20, M_CNT_W = 21, M_MS = 22, M_EP = 24, M_PDONE = 26, M_CNT_R = 27, M_TS = 32 };

__device__ __forceinline__ float u2f(unsigned u) { return __builtin_bit_cast(float, u); }
__device__ __forceinline__ unsigned f2u(float f) { return __builtin_bit_cast(unsigned, f); }
__device__ __forceinline__ float rbf(float f) { return (float)(__bf16)f; }
__device__ __forceinline__ unsigned pk2(float lo, float hi) { bf16x2 v = {(__bf16)lo, (__bf16)hi}; return __builtin_bit_cast(unsigned, v); }
__device__ __forceinline__ float blo(unsigned u) { return u2f(u << 16); }
__device__ __forceinline__ float bhi(unsigned u) { return u2f(u & 0xffff0000u); }
__device__ __forceinline__ float dot2(unsigned a, unsigned b, float c) {
  return __builtin_amdgcn_fdot2_f32_bf16(__builtin_bit_cast(bf16x2, a), __builtin_bit_cast(bf16x2, b), c, false);
}
template <int CTRL> __device__ __forceinline__ float dpp(float x) {
  return __builtin_bit_cast(float, __builtin_amdgcn_mov_dpp(__builtin_bit_cast(int, x), CTRL, 0xf, 0xf, true));
}
constexpr int XOR1 = 0xB1, XOR2 = 0x4E, XOR7 = 0x141, ROR8 = 0x128, ROR4 = 0x124;
__device__ __forceinline__ float swap16_add(float a, float b) {
  auto s = __builtin_amdgcn_permlane16_swap(f2u(a), f2u(b), false, false);
  const unsigned s0 = s[0], s1 = s[1];
  return u2f(s0) + u2f(s1);
}
__device__ __forceinline__ float swap32_add(float a, float b) {
  auto s = __builtin_amdgcn_permlane32_swap(f2u(a), f2u(b), false, false);
  const unsigned s0 = s[0], s1 = s[1];
  return u2f(s0) + u2f(s1);
}
__device__ __forceinline__ float swap16_max(float a) {
  auto s = __builtin_amdgcn_permlane16_swap(f2u(a), f2u(a), false, false);
  const unsigned s0 = s[0], s1 = s[1];
  return fmaxf(u2f(s0), u2f(s1));
}
__device__ __forceinline__ float swap32_max(float a) {
  auto s = __builtin_amdgcn_permlane32_swap(f2u(a), f2u(a), false, false);
  const unsigned s0 = s[0], s1 = s[1];
  return fmaxf(u2f(s0), u2f(s1));
}
__device__ __forceinline__ float row_sum(float x) { x += dpp<ROR8>(x); x += dpp<ROR4>(x); x += dpp<XOR2>(x); x += dpp<XOR1>(x); return x; }
__device__ __forceinline__ float row_max(float x) { x = fmaxf(x, dpp<ROR8>(x)); x = fmaxf(x, dpp<ROR4>(x)); x = fmaxf(x, dpp<XOR2>(x)); x = fmaxf(x, dpp<XOR1>(x)); return x; }
__device__ __forceinline__ float wave_sum(float x) { x = row_sum(x); x = swap16_add(x, x); return swap32_add(x, x); }
__device__ __forceinline__ float wave_max(float x) { x = row_max(x); x = swap16_max(x); return swap32_max(x); }

__device__ __forceinline__ __amdgpu_buffer_rsrc_t mkrs(const void* p, unsigned bytes) { return __builtin_amdgcn_make_buffer_rsrc((void*)p, 0, bytes, 0x00020000); }
#define LD128(rs, vo, so, aux) __builtin_amdgcn_raw_buffer_load_b128(rs, vo, so, aux)
#define LD64(rs, vo, so, aux) __builtin_amdgcn_raw_buffer_load_b64(rs, vo, so, aux)
#define ST128(v, rs, vo, so, aux) __builtin_amdgcn_raw_buffer_store_b128(v, rs, vo, so, aux)
#define ST64(v, rs, vo, so, aux) __builtin_amdgcn_raw_buffer_store_b64(v, rs, vo, so, aux)
#define ST32(v, rs, vo, so, aux) __builtin_amdgcn_raw_buffer_store_b32(v, rs, vo, so, aux)
#define PIN() asm volatile("" ::: "memory")
#define USE(x) asm volatile("" :: "v"(x) : "memory")
#define REDEF(x) asm volatile("" : "+v"(x) :: "memory")
#define BARRIER() asm volatile("s_waitcnt lgkmcnt(0)\n\ts_barrier" ::: "memory")
#define DRAIN() asm volatile("s_waitcnt vmcnt(0)" ::: "memory")
#define LGKM0() asm volatile("s_waitcnt lgkmcnt(0)" ::: "memory")
constexpr int SPIN_CAP = 1 << 16;
constexpr int LSPIN_CAP = 1 << 20;

#define STAMP(i) do {} while (0)
#define RET() return

__device__ __forceinline__ void ln8(float (&v)[8], int lane) {
  float s = ((v[0] + v[1]) + (v[2] + v[3])) + ((v[4] + v[5]) + (v[6] + v[7]));
  const float mean = wave_sum(s) * (1.0f / DM);
  float q = 0.f;
#pragma unroll
  for (int e = 0; e < 8; ++e) { const float d = v[e] - mean; q = fmaf(d, d, q); }
  q = lane < 56 ? q : 0.f;
  const float var = wave_sum(q) * (1.0f / DM);
  const float mb = rbf(mean);
  const float den = rbf(__builtin_amdgcn_sqrtf(rbf(rbf(var) + 1e-5f)));
  const float inv = __builtin_amdgcn_rcpf(den);
#pragma unroll
  for (int e = 0; e < 8; ++e) v[e] = rbf(v[e] - mb) * inv;
}
__device__ __forceinline__ float reduce_pieces(f32x4 v) {
  const float a = swap32_add(v[0], v[2]), b = swap32_add(v[1], v[3]);
  float c = swap16_add(a, b);
  c += dpp<ROR8>(c); c += dpp<ROR4>(c); c += dpp<XOR2>(c);
  return c;
}
__device__ __forceinline__ v4u enc4(f32x4 v, unsigned tag) {
  v4u r;
  r[0] = __builtin_amdgcn_perm(f2u(v[0]), tag, 0x07060500u); r[1] = __builtin_amdgcn_perm(f2u(v[1]), tag, 0x07060501u);
  r[2] = __builtin_amdgcn_perm(f2u(v[2]), tag, 0x07060502u); r[3] = __builtin_amdgcn_perm(f2u(v[3]), tag, 0x07060503u);
  return r;
}
__device__ __forceinline__ bool valid4(v4u a, unsigned tag) {
  return (__builtin_amdgcn_perm(a[1], a[0], 0x0c0c0400u) | __builtin_amdgcn_perm(a[3], a[2], 0x04000c0cu)) == tag;
}
__device__ __forceinline__ f32x4 dec4(v4u a) { return __builtin_bit_cast(f32x4, (a & 0xFFFFFF00u) | 0x80u); }
__device__ __forceinline__ f32x4 dec4x(v4u a) { return __builtin_bit_cast(f32x4, a & 0xFFFFFF00u); }

#define TS(i) do {} while (0)
#define SPIN(cap) _Pragma("unroll 1") for (int spin = 0; spin < (cap); ++spin)
#define LDS_INC(w) __hip_atomic_fetch_add((LAS unsigned*)(smem + L_MISC) + (w), 1u, __ATOMIC_RELAXED, __HIP_MEMORY_SCOPE_WORKGROUP)
#define LDS_WAIT_GE(w, n, slp) do { SPIN(LSPIN_CAP) { PIN(); if (misc[w] >= (unsigned)(n)) break; if (slp) __builtin_amdgcn_s_sleep(slp); } PIN(); } while (0)
template <int CTRL> __device__ __forceinline__ float comb(bool bit, float lo, float hi) {
  const float a = lo + dpp<CTRL>(lo), b = hi + dpp<CTRL>(hi);
  return bit ? b : a;
}
__device__ __forceinline__ f32x4 sel4(bool s1, bool s2, f32x4 a, f32x4 b, f32x4 c, f32x4 d) {
  f32x4 r;
#pragma unroll
  for (int k = 0; k < 4; ++k) { const float lo = s1 ? b[k] : a[k], hi = s1 ? d[k] : c[k]; r[k] = s2 ? hi : lo; }
  return r;
}
__device__ __forceinline__ bf16x4 trd(const LAS unsigned char* p) { return __builtin_amdgcn_ds_read_tr16_b64_v4bf16((LAS bf16x4*)p); }
__device__ __forceinline__ unsigned now32() { return (unsigned)__builtin_amdgcn_s_memrealtime(); }

__global__ void __launch_bounds__(NTHR, 2)
fused_decode(const u16* x, const u16* Kc, const u16* Vc, const u16* wq, const u16* wo, const u16* w1, const u16* w2, u16* out, unsigned char* ws) {
  __shared__ __attribute__((aligned(16))) unsigned char smem[L_BYTES];
  const int tid = threadIdx.x, lane = tid & 63;
  int wave = __builtin_amdgcn_readfirstlane(tid >> 6);
  const int g = blockIdx.x;
  const int b = g >> 6, h = (g >> 2) & 15, sp = g & 3;
  const int half = lane >> 5, c32 = lane & 31, g4 = lane >> 4, c16 = lane & 15;
  volatile LAS unsigned* misc = (volatile LAS unsigned*)(smem + L_MISC);
  const __amdgpu_buffer_rsrc_t rws = mkrs(ws, WS_BYTES);
  const __amdgpu_buffer_rsrc_t rk = mkrs(Kc + (size_t)g * (2048 * DH), 2048 * DH * 2);
  const __amdgpu_buffer_rsrc_t rx = mkrs(x, NB * DM * 2);
  const int voff = tid * 16;
  const int hw = wave - 4;
  v4u kb[PRE];
  u64 pc0;
  asm volatile("s_getpc_b64 %0\n.Lfd_code_begin:" : "=s"(pc0));
  unsigned clen, clen2;
  asm volatile("s_mov_b32 %0, .Lfd_code_end-.Lfd_code_begin\n\ts_mov_b32 %1, .Lfd_code_end2-.Lfd_code_begin" : "=s"(clen), "=s"(clen2));
  clen = clen > clen2 ? clen : clen2;
  const unsigned tstart = now32() + PACE2_OFF;
  if (wave < 4) {
#pragma unroll
    for (int i = 0; i < WIN; ++i) kb[i] = LD128(rk, voff, i * 4096, AUX_K);
#pragma unroll
    for (int i = WIN; i < 12; ++i) asm volatile("" : "=v"(kb[i]));
  } else {
    const __amdgpu_buffer_rsrc_t rq = mkrs(wq + h * DH + (sp * 4 + b) * 16, LEVEL < 1 ? 0 : (DM - 1) * (NH * DH * 2) + 32);
    kb[0] = LD128(rx, lane < 56 ? lane * 16 : (int)OOB, hw * (DM * 2), 0);
#pragma unroll
    for (int i = 0; i < 4; ++i) kb[1 + i] = LD128(rq, (lane >> 1) * (NH * DH * 2) + (lane & 1) * 16, (hw + 4 * i) * 32 * (NH * DH * 2), AUX_W);
    kb[5] = LD128(rws, 0, WS_EPOCH, 0);
    {
      const __amdgpu_buffer_rsrc_t rc = mkrs((const void*)pc0, clen);
#pragma unroll
      for (int i = 0; i < 6; ++i) kb[6 + i] = LD128(rc, voff - 4096, i * 4096, 0);
    }
#pragma unroll
    for (int i = 12; i < WIN; ++i) asm volatile("" : "=v"(kb[i]));
    if (wave == 4 && lane >= 12 && lane < 32) misc[lane] = 0u;
  }
  BARRIER();
  asm volatile("" : "+s"(wave));

  const int unit = g & 63;
  const int oct = (g >> 7) * 16 + h, pj = (b & 1) * 4 + sp;
  if (wave < 4) {
    const __amdgpu_buffer_rsrc_t rv = mkrs(Vc + (size_t)g * (2048 * DH), 2048 * DH * 2);
#pragma unroll
    for (int i = WIN; i < PRE; ++i) { USE(kb[i - WIN]); kb[i] = LD128(rk, voff, i * 4096, AUX_K); PIN(); }
    STAMP(1);
    BARRIER();
    STAMP(2);
    const v4u qv = *(const v4u*)(smem + L_QB + c32 * 16);
    const unsigned tag = __builtin_amdgcn_readfirstlane(misc[M_TAG]);
    float* sc = (float*)(smem + L_SC) + (wave * 2 + half) * 256;
    float* scw = sc + (((c32 >> 4) & 1) | ((c32 >> 2) & 2) | (c32 & 4) | ((c32 << 2) & 8));
    const bool b3 = c32 & 8, b2 = c32 & 4, b1 = c32 & 2;
    float m = -INFINITY;
    float t0_, t1_, t2_, t3_;
#define COMB(CTRL, BIT, lo, hi) comb<CTRL>(BIT, lo, hi)
#define KSTEP(v, i, grp) do { \
      float d_ = dot2((v)[0], qv[0], 0.f); d_ = dot2((v)[1], qv[1], d_); d_ = dot2((v)[2], qv[2], d_); d_ = dot2((v)[3], qv[3], d_); \
      if (((i) & 1) == 0) t0_ = d_; else { d_ = swap16_add(t0_, d_); \
        if (((i) & 2) == 0) t1_ = d_; else { d_ = COMB(ROR8, b3, t1_, d_); \
          if (((i) & 4) == 0) t2_ = d_; else { d_ = COMB(XOR7, b2, t2_, d_); \
            if (((i) & 8) == 0) t3_ = d_; else { d_ = COMB(XOR2, b1, t3_, d_); d_ += dpp<XOR1>(d_); \
              d_ = rbf(d_) * 0.0625f; m = fmaxf(m, d_); scw[(grp) * 16] = d_; } } } } \
      asm volatile("" : "+v"(t0_), "+v"(t1_), "+v"(t2_), "+v"(t3_), "+v"(m)); } while (0)
#define PACE2_AT(row) do {} while (0)
    asm volatile("" : "=v"(t0_), "=v"(t1_), "=v"(t2_), "=v"(t3_));
#pragma unroll
    for (int i = 0; i < PRE; ++i) {
      REDEF(kb[i]);
      KSTEP(kb[i], i & 15, i >> 4);
      if (i < WIN) { PIN(); kb[i] = LD128(rk, voff, (PRE + i) * 4096, AUX_K); PIN(); }
    }
    TS(0);
#pragma unroll 1
    for (int gi = PRE / 16; gi < 16; ++gi) {
      const __amdgpu_buffer_rsrc_t rn = gi < 15 ? rk : rv;
      const int offn = gi < 15 ? (gi + 1) * 16 * 4096 : 0;
#pragma unroll
      for (int i = 0; i < 16; ++i) {
        if ((i & (PACE2_STEP - 1)) == 0) PACE2_AT(gi * 16 + i);
        USE(kb[i]);
        if (i + WIN < 16) kb[i + WIN] = LD128(rk, voff, (gi * 16 + i + WIN) * 4096, AUX_K);
        else kb[i + WIN - 16] = LD128(rn, voff, offn + (i + WIN - 16) * 4096, AUX_K);
        REDEF(kb[i]);
        KSTEP(kb[i], i, gi);
      }
      if (gi & 1) TS(gi >> 1);
    }
    STAMP(3);
    m = wave_max(m);
    {
      LGKM0();
      f32x4* ps = (f32x4*)(smem + L_SC) + wave * 128 + lane * 2;
      f32x4 a = ps[0], bq = ps[1];
      const float mm = m * LOG2E;
#pragma unroll
      for (int e = 0; e < 4; ++e) { a[e] = __builtin_amdgcn_exp2f(fmaf(a[e], LOG2E, -mm)); bq[e] = __builtin_amdgcn_exp2f(fmaf(bq[e], LOG2E, -mm)); }
      ps[0] = a; ps[1] = bq;
      float l = ((a[0] + a[1]) + (a[2] + a[3])) + ((bq[0] + bq[1]) + (bq[2] + bq[3]));
      l = wave_sum(l);
      if (lane == 0) { misc[M_ML + 2 * wave] = f2u(m); misc[M_ML + 2 * wave + 1] = f2u(l); }
      LGKM0();
      if (lane == 0) LDS_INC(M_CNT_ML);
    }
    float acc[8], wgt = 0.f;
#pragma unroll
    for (int k = 0; k < 8; ++k) acc[k] = 0.f;
#define VBODY(gi, LAST) do { \
      f32x4 p4[4]; \
      _Pragma("unroll") for (int i = 0; i < 4; ++i) p4[i] = *(const f32x4*)(sc + (gi) * 16 + i * 4); \
      TS(8 + (gi)); \
      _Pragma("unroll") for (int i = 0; i < 16; ++i) { \
        if ((i & (PACE2_STEP - 1)) == 0) PACE2_AT(256 + (gi) * 16 + i); \
        if ((LAST) && i == LATE_I && tid == 0) misc[M_LATE] = 1u; \
        USE(kb[i]); \
        if (!(LAST) || i + WIN < 16) kb[(i + WIN) & 15] = LD128(rv, voff, ((gi) * 16 + i + WIN) * 4096, AUX_NT); \
        REDEF(kb[i]); \
        const float p = p4[i >> 2][i & 3]; \
        _Pragma("unroll") for (int k = 0; k < 4; ++k) { acc[2 * k] = fmaf(p, blo(kb[i][k]), acc[2 * k]); acc[2 * k + 1] = fmaf(p, bhi(kb[i][k]), acc[2 * k + 1]); } \
        asm volatile("" : "+v"(acc[0]), "+v"(acc[1]), "+v"(acc[2]), "+v"(acc[3]), "+v"(acc[4]), "+v"(acc[5]), "+v"(acc[6]), "+v"(acc[7])); \
      } } while (0)
#pragma unroll 1
    for (int gi = 0; gi < 15; ++gi) VBODY(gi, false);
    LDS_WAIT_GE(M_WOK, 1, 2);
    wgt = u2f(misc[M_WGT + wave]);
    VBODY(15, true);
    STAMP(4);
    {
      f32x4 o;
#pragma unroll
      for (int k = 0; k < 4; ++k) o[k] = swap32_add(acc[k], acc[k + 4]) * wgt;
      f32x4* oa = (f32x4*)(smem + L_OA) + lane;
      oa[wave * 64] = o;
      LGKM0();
      unsigned old = 0;
      if (lane == 0) old = LDS_INC(M_CNT_O);
      if (__builtin_amdgcn_readfirstlane(old) == 3u) {
        PIN();
        o = (oa[0] + oa[64]) + (oa[128] + oa[192]);
        ST128(enc4(o, tag), rws, (c32 >> 2) * 1024 + (c32 & 3) * 32 + half * 16, WS_OX + oct * 8192 + pj * 128, AUX_SC1);
      }
    }
    STAMP(5);
    const int ru = unit - RU0;
    if (ru < 0 || ru >= NUNIT) RET();
    const v2u xr = LD64(rx, (lane & 7) * 8, b * (DM * 2) + ru * 64, 0);
    const int po = (lane >> 3) * (DM * 4) + (lane & 7) * 16;
    float* rs = (float*)(smem + L_RS);
    LDS_WAIT_GE(M_PDONE, 1, 1);
#pragma unroll 1
    for (int ph = 0; ph < 2; ++ph) {
      const unsigned ib = (ph == 0 ? WS_Y + (unsigned)(b * 128 + wave * 32) * (DM * 4u) : WS_O + (unsigned)(b * NM + wave * 28) * (DM * 4u)) + ru * 128;
      v4u p0 = {0u, 0u, 0u, 0u}, p1 = p0, p2 = p0, p3 = p0;
      bool k0 = false, k1 = false, k2 = false, k3 = ph == 1 && lane >= 32;
      SPIN(SPIN_CAP) {
        PIN();
        const v4u n0 = LD128(rws, k0 ? (int)OOB : po, ib, AUX_SC1), n1 = LD128(rws, k1 ? (int)OOB : po, ib + 8 * DM * 4, AUX_SC1);
        const v4u n2 = LD128(rws, k2 ? (int)OOB : po, ib + 16 * DM * 4, AUX_SC1), n3 = LD128(rws, k3 ? (int)OOB : po, ib + 24 * DM * 4, AUX_SC1);
        p0 = k0 ? p0 : n0; p1 = k1 ? p1 : n1; p2 = k2 ? p2 : n2; p3 = k3 ? p3 : n3;
        k0 = valid4(p0, tag); k1 = valid4(p1, tag); k2 = valid4(p2, tag); k3 |= valid4(p3, tag);
        if (__all(k0 & k1 & k2 & k3)) break;
      }
      if (ph == 0 || (wave & 1)) STAMP(6 + ph);
      const f32x4 v = (dec4(p0) + dec4(p1)) + (dec4(p2) + dec4(p3));
      float c = swap16_add(swap32_add(v[0], v[2]), swap32_add(v[1], v[3]));
      c += dpp<ROR8>(c);
      rs[wave * 32 + (lane & 7) * 4 + 2 * half + (g4 & 1)] = c;
      LGKM0();
      unsigned old = 0;
      if (lane == 0) old = LDS_INC(M_CNT_R);
      if (__builtin_amdgcn_readfirstlane(old) == 3u + 4u * ph) {
        PIN();
        const f32x4* r4 = (const f32x4*)rs + (lane & 7);
        const f32x4 sum = (r4[0] + r4[8]) + (r4[16] + r4[24]);
        f32x4 res;
        if (ph == 0) {
          res[0] = rbf(blo(xr[0]) + rbf(sum[0])); res[1] = rbf(bhi(xr[0]) + rbf(sum[1])); res[2] = rbf(blo(xr[1]) + rbf(sum[2])); res[3] = rbf(bhi(xr[1]) + rbf(sum[3]));
          ST128(enc4(res, tag), rws, (lane >> 3) * (NB * DM * 4) + (lane & 7) * 16, WS_HB + b * (DM * 4) + ru * 128, AUX_SC1);
          *((f32x4*)rs + 32 + (lane & 7)) = res;
          LGKM0();
          if (lane == 0) misc[M_RDONE] = 1u;
        } else {
          const f32x4 hq = r4[32];
          const v2u ov = {pk2(hq[0] + rbf(sum[0]), hq[1] + rbf(sum[1])), pk2(hq[2] + rbf(sum[2]), hq[3] + rbf(sum[3]))};
          ST64(ov, mkrs(out, NB * DM * 2), lane < 8 ? lane * 8 : (int)OOB, b * (DM * 2) + ru * 64, 0);
          if (g == RU0) {
            const v2u ne = {misc[M_EP], misc[M_EP + 1]};
            ST64(ne, rws, lane == 0 ? 0 : (int)OOB, WS_EPOCH, AUX_SC1);
          }
        }
      }
      if (ph == 0) LDS_WAIT_GE(M_RDONE, 1, 1);
    }
    STAMP(1);
    asm volatile(".Lfd_code_end2:");
    RET();
  }
  const bool is_m = unit < 28;
  const int mi = unit * 4 + b;
  const int tq = c16 >> 2, tp = c16 & 3;
  const LAS unsigned char* s1 = (const LAS unsigned char*)(smem + L_W1) + (g4 * 8 + tq) * 32 + tp * 8;
  const unsigned char* hp = smem + L_HN + (c16 & 3) * HN_STRIDE + g4 * 16;
  {
    float v[8];
#pragma unroll
    for (int k = 0; k < 4; ++k) { v[2 * k] = blo(kb[0][k]); v[2 * k + 1] = bhi(kb[0][k]); }
    ln8(v, lane);
    const v4u xn = {pk2(v[0], v[1]), pk2(v[2], v[3]), pk2(v[4], v[5]), pk2(v[6], v[7])};
    if (lane < 56) *(v4u*)(smem + L_HN + hw * HN_STRIDE + lane * 16) = xn;
#pragma unroll
    for (int i = 0; i < 4; ++i) if (hw + 4 * i < 14) *(v4u*)(smem + L_W1 + (hw + 4 * i) * 1024 + lane * 16) = kb[1 + i];
    LGKM0();
    if (lane == 0) LDS_INC(M_CNT_Q);
  }
  const unsigned e0 = __builtin_amdgcn_readfirstlane(kb[5][0]), e1 = __builtin_amdgcn_readfirstlane(kb[5][1]);
  unsigned qtag = e0 + 0x9E3779B9u;
  qtag ^= qtag >> 16; qtag *= 0x85EBCA6Bu; qtag ^= qtag >> 13; qtag *= 0xC2B2AE35u; qtag ^= qtag >> 16;
  if (hw == 0) {
    LDS_WAIT_GE(M_CNT_Q, 4, 0);
    bf16x8 a1[14], bfr[14];
#pragma unroll
    for (int s = 0; s < 14; ++s) {
      a1[s] = __builtin_shufflevector(trd(s1 + s * 1024), trd(s1 + s * 1024 + 128), 0, 1, 2, 3, 4, 5, 6, 7);
      bfr[s] = *(const bf16x8*)(hp + s * 64);
    }
    __builtin_amdgcn_sched_barrier(0);
    f32x4 da = {0.f, 0.f, 0.f, 0.f}, db = {0.f, 0.f, 0.f, 0.f};
#pragma unroll
    for (int s = 0; s < 14; s += 2) {
      da = __builtin_amdgcn_mfma_f32_16x16x32_bf16(a1[s], bfr[s], da, 0, 0, 0);
      db = __builtin_amdgcn_mfma_f32_16x16x32_bf16(a1[s + 1], bfr[s + 1], db, 0, 0, 0);
    }
    da += db;
    const v4u qg = {pk2(da[0], da[1]), qtag, pk2(da[2], da[3]), qtag};
    ST128(qg, rws, c16 < 4 ? c16 * 64 + g4 * 16 : (int)OOB, WS_Q + h * 4096 + (sp * 4 + b) * 256, AUX_SC1);
    if (lane == 0) LDS_INC(M_CNT_Q);
    STAMP(1);
  } else if (hw == 3) {
    if (lane == 0) { misc[M_TAG] = qtag; misc[M_EP] = e0 + 1u; misc[M_EP + 1] = e1; }
  } else if (hw == 1) {
    v4u qg;
    LDS_WAIT_GE(M_CNT_Q, 5, 1);
    SPIN(SPIN_CAP) {
      PIN();
      qg = LD128(rws, (lane >> 2) * 256 + (lane & 3) * 16, WS_Q + h * 4096 + b * 64, AUX_SC1);
      if (__all(qg[1] == qtag && qg[3] == qtag)) break;
    }
    *(v2u*)(smem + L_QB + lane * 8) = v2u{qg[0], qg[2]};
    STAMP(1);
  }
#pragma unroll
  for (int i = 6; i < 12; ++i) USE(kb[i]);
  BARRIER();
  bf16x8 a0[8];
  {
    const __amdgpu_buffer_rsrc_t r0 = mkrs(wo + (size_t)(h * DH + pj * 32) * DM, 32 * DM * 2);
    v4u t0[7];
#pragma unroll
    for (int i = 0; i < 7; ++i) t0[i] = LD128(r0, lane * 16, (hw + 4 * i) * 1024, AUX_W);
    const unsigned d1 = __builtin_amdgcn_raw_buffer_load_b16(mkrs(hw == 0 ? w1 : hw == 1 ? w2 : hw == 2 ? w1 + DM * FF - 64 : w2 + DM * FF - 64, 64), 0, 0, 0);
#pragma unroll
    for (int i = 0; i < 7; ++i) *(v4u*)(smem + L_WO + (hw + 4 * i) * 1024 + lane * 16) = t0[i];
    USE(d1);
    LGKM0();
    if (lane == 0) LDS_INC(M_CNT_W);
    LDS_WAIT_GE(M_CNT_W, 4, 2);
    const LAS unsigned char* s0 = (const LAS unsigned char*)(smem + L_WO) + (g4 * 8 + tq) * (DM * 2) + (hw * 64 + tp * 4) * 2;
#pragma unroll
    for (int t = 0; t < 8; ++t) a0[t] = __builtin_shufflevector(trd(s0 + (t & 3) * 32 + (t >> 2) * 512), trd(s0 + (t & 3) * 32 + (t >> 2) * 512 + 4 * DM * 2), 0, 1, 2, 3, 4, 5, 6, 7);
#pragma unroll
    for (int t = 0; t < 8; ++t) asm volatile("" : "+v"(a0[t]));
  }
  if (hw == 0) {
    LDS_WAIT_GE(M_CNT_ML, 4, 16);
    const float mo = u2f(misc[M_ML + 2 * (lane & 3)]), lo = u2f(misc[M_ML + 2 * (lane & 3) + 1]);
    float M = fmaxf(mo, dpp<XOR1>(mo)); M = fmaxf(M, dpp<XOR2>(M));
    const float e = __builtin_amdgcn_exp2f((mo - M) * LOG2E);
    float L = lo * e; L += dpp<XOR1>(L); L += dpp<XOR2>(L);
    if (lane < 4) misc[M_WGT + lane] = f2u(e);
    if (lane == 0) { misc[M_MS] = f2u(M); misc[M_MS + 1] = f2u(L); }
    LGKM0();
    if (lane == 0) misc[M_WOK] = 1u;
    const unsigned dummy = __builtin_amdgcn_raw_buffer_load_b16(mkrs(out, NB * DM * 2), 0, 0, 0);
    USE(dummy);
  }
  STAMP(4);
  LDS_WAIT_GE(M_LATE, 1, 4);
  if (hw == 0) {
    const f32x4 mlv = {u2f(misc[M_MS]), u2f(misc[M_MS + 1]), 0.f, 0.f};
    ST128(enc4(mlv, qtag), rws, lane < 8 ? lane * 1024 : (int)OOB, WS_ML + oct * 8192 + pj * 128, AUX_SC1);
  }
  v4u t1[4], t2[4];
  {
    const __amdgpu_buffer_rsrc_t r1 = mkrs(w1 + mi * 16, is_m ? (DM - 1) * (FF * 2) + 32 : 0);
    const __amdgpu_buffer_rsrc_t r2 = mkrs(w2 + (size_t)mi * 16 * DM, is_m ? 16 * DM * 2 : 0);
#pragma unroll
    for (int i = 0; i < 4; ++i) {
      t1[i] = LD128(r1, (lane >> 1) * (FF * 2) + (lane & 1) * 16, (hw + 4 * i) * 32 * (FF * 2), AUX_W);
      t2[i] = LD128(r2, lane * 16, (hw + 4 * i) * 1024, AUX_W);
    }
    PIN();
  }
  STAMP(3);
  {
    v4u pv;
    SPIN(SPIN_CAP) {
      PIN();
      pv = LD128(rws, (c16 & 7) * 128, WS_ML + oct * 8192 + pj * 1024, AUX_SC1);
      if (__all(valid4(pv, qtag))) break;
      __builtin_amdgcn_s_sleep(1);
    }
    float wn;
    {
      const float ms = u2f(pv[0] & 0xFFFFFF00u), ls = u2f((pv[1] & 0xFFFFFF00u) | 0x80u);
      float M = fmaxf(ms, dpp<XOR1>(ms)); M = fmaxf(M, dpp<XOR2>(M));
      const float e = __builtin_amdgcn_exp2f((ms - M) * LOG2E);
      float L = ls * e; L += dpp<XOR1>(L); L += dpp<XOR2>(L);
      wn = e * __builtin_amdgcn_rcpf(L);
    }
    const int po = ((c16 >> 2) & 1) * 512 + (c16 & 3) * 128 + g4 * 32 + (c16 >> 3) * 16;
    SPIN(SPIN_CAP) {
      PIN();
      pv = LD128(rws, po, WS_OX + oct * 8192 + pj * 1024, AUX_SC1);
      if (__all(valid4(pv, qtag))) break;
    }
    STAMP(5);
    f32x4 o = dec4(pv);
#pragma unroll
    for (int k = 0; k < 4; ++k) { o[k] *= wn; o[k] += dpp<XOR1>(o[k]); o[k] += dpp<XOR2>(o[k]); }
    v4u bo;
    bo[0] = pk2(o[0], o[1]); bo[1] = pk2(o[2], o[3]);
    bo[2] = f2u(dpp<ROR8>(u2f(bo[0]))); bo[3] = f2u(dpp<ROR8>(u2f(bo[1])));
    f32x4 y[8];
#pragma unroll
    for (int t = 0; t < 8; ++t) y[t] = __builtin_amdgcn_mfma_f32_16x16x32_bf16(a0[t], __builtin_bit_cast(bf16x8, bo), f32x4{0.f, 0.f, 0.f, 0.f}, 0, 0, 0);
    __builtin_amdgcn_sched_barrier(0);
    const int vo = c16 < 8 ? (c16 >> 2) * (128 * DM * 4) + (c16 & 3) * 64 + g4 * 16 : (int)OOB;
    const bool s1b = c16 & 1, s2b = c16 & 2;
    const unsigned yb = WS_Y + (unsigned)((b & 2) * 128 + h * 8 + pj) * (DM * 4u) + hw * 256;
    ST128(enc4(sel4(s1b, s2b, y[0], y[1], y[2], y[3]), qtag), rws, vo, yb, AUX_SC1);
    ST128(enc4(sel4(s1b, s2b, y[4], y[5], y[6], y[7]), qtag), rws, hw < 3 ? vo : (int)OOB, yb + 1024, AUX_SC1);
    if (lane == 0) misc[M_PDONE] = 1u;
  }
  STAMP(6);
  if (!is_m) RET();
  bf16x8 a1[14];
  s16x4 a2[8];
  {
#pragma unroll
    for (int i = 0; i < 4; ++i) if (hw + 4 * i < 14) {
      *(v4u*)(smem + L_W1 + (hw + 4 * i) * 1024 + lane * 16) = t1[i];
      *(v4u*)(smem + L_W2 + (hw + 4 * i) * 1024 + lane * 16) = t2[i];
    }
    LGKM0();
    if (lane == 0) LDS_INC(M_CNT_W);
    LDS_WAIT_GE(M_CNT_W, 8, 0);
    const LAS unsigned char* s2 = (const LAS unsigned char*)(smem + L_W2) + (g4 * 4 + tq) * (DM * 2) + (hw * 64 + tp * 4) * 2;
#pragma unroll
    for (int t = 0; t < 8; ++t) a2[t] = __builtin_bit_cast(s16x4, trd(s2 + (t & 3) * 32 + (t >> 2) * 512));
    asm volatile("" : "+v"(a2[7]));
#pragma unroll
    for (int s = 0; s < 14; ++s) a1[s] = __builtin_shufflevector(trd(s1 + s * 1024), trd(s1 + s * 1024 + 128), 0, 1, 2, 3, 4, 5, 6, 7);
#pragma unroll
    for (int t = 0; t < 7; ++t) asm volatile("" : "+v"(a2[t]), "+v"(a1[2 * t]), "+v"(a1[2 * t + 1]));
  }
  v4u h0, h1;
  {
    const unsigned hb = WS_HB + (unsigned)(g & 7) * (NB * DM * 4) + hw * (DM * 4);
    const int vo = lane < 56 ? lane * 32 : (int)OOB;
    SPIN(SPIN_CAP) {
      PIN();
      h0 = LD128(rws, vo, hb, AUX_SC1); h1 = LD128(rws, vo, hb + 16, AUX_SC1);
      const bool ok = (valid4(h0, qtag) & valid4(h1, qtag)) | (lane >= 56);
      if (__all(ok)) break;
    }
  }
  STAMP(7);
  {
    float v[8];
#pragma unroll
    for (int k = 0; k < 4; ++k) { v[k] = bhi(h0[k]); v[k + 4] = bhi(h1[k]); }
    ln8(v, lane);
    const v4u hn = {pk2(v[0], v[1]), pk2(v[2], v[3]), pk2(v[4], v[5]), pk2(v[6], v[7])};
    if (lane < 56) *(v4u*)(smem + L_HN + hw * HN_STRIDE + lane * 16) = hn;
    LGKM0();
    if (lane == 0) LDS_INC(M_CNT_HN);
    LDS_WAIT_GE(M_CNT_HN, 4, 0);
  }
  f32x4 d1a = {0.f, 0.f, 0.f, 0.f}, d1b = {0.f, 0.f, 0.f, 0.f};
  {
    bf16x8 bfr[14];
#pragma unroll
    for (int s = 0; s < 14; ++s) bfr[s] = *(const bf16x8*)(hp + s * 64);
    __builtin_amdgcn_sched_barrier(0);
#pragma unroll
    for (int s = 0; s < 14; s += 2) {
      d1a = __builtin_amdgcn_mfma_f32_16x16x32_bf16(a1[s], bfr[s], d1a, 0, 0, 0);
      d1b = __builtin_amdgcn_mfma_f32_16x16x32_bf16(a1[s + 1], bfr[s + 1], d1b, 0, 0, 0);
    }
  }
  float sv[4];
#pragma unroll
  for (int r = 0; r < 4; ++r) { const float t = rbf(d1a[r] + d1b[r]); sv[r] = t * __builtin_amdgcn_rcpf(1.0f + __builtin_amdgcn_exp2f(-t * LOG2E)); }
  const v2u hbp = {pk2(sv[0], sv[1]), pk2(sv[2], sv[3])};
  const s16x4 b2v = __builtin_bit_cast(s16x4, hbp);
  const int vo = (c16 & 3) * (NM * DM * 4) + (c16 >> 2) * 64 + g4 * 16;
  f32x4 o2[8];
#pragma unroll
  for (int t = 0; t < 8; ++t) o2[t] = __builtin_amdgcn_mfma_f32_16x16x16bf16_1k(a2[t], b2v, f32x4{0.f, 0.f, 0.f, 0.f}, 0, 0, 0);
  __builtin_amdgcn_sched_barrier(0);
  {
    const bool s1b = c16 & 4, s2b = c16 & 8;
    const unsigned ob = WS_O + (unsigned)mi * (DM * 4u) + hw * 256;
    ST128(enc4(sel4(s1b, s2b, o2[0], o2[1], o2[2], o2[3]), qtag), rws, vo, ob, AUX_SC1);
    ST128(enc4(sel4(s1b, s2b, o2[4], o2[5], o2[6], o2[7]), qtag), rws, hw < 3 ? vo : (int)OOB, ob + 1024, AUX_SC1);
  }
  STAMP(2);
  asm volatile(".Lfd_code_end:");
  RET();
}

extern "C" void kernel_launch(void* const* d_in, const int* in_sizes, int n_in, void* d_out, int out_size, void* d_ws, size_t ws_size, hipStream_t stream) {
  fused_decode<<<NWG, NTHR, 0, stream>>>((const u16*)d_in[0], (const u16*)d_in[1], (const u16*)d_in[2], (const u16*)d_in[3], (const u16*)d_in[4],
                                         (const u16*)d_in[6], (const u16*)d_in[7], (u16*)d_out, (unsigned char*)d_ws);
}
```

```cpp
#define STAMPS 1
#define PROBE 0
#include <hip/hip_runtime.h>
#include <stdint.h>

typedef unsigned v4u __attribute__((ext_vector_type(4)));
typedef unsigned v2u __attribute__((ext_vector_type(2)));
typedef float f32x4 __attribute__((ext_vector_type(4)));
typedef __bf16 bf16x2 __attribute__((ext_vector_type(2)));
typedef __bf16 bf16x4 __attribute__((ext_vector_type(4)));
typedef __bf16 bf16x8 __attribute__((ext_vector_type(8)));
typedef short s16x4 __attribute__((ext_vector_type(4)));
typedef unsigned short u16;
typedef unsigned long long u64;
#define LAS __attribute__((address_space(3)))

constexpr int NB = 4, DM = 448, NH = 16, SK = 8192, DH = 256, FF = 1792;
constexpr int NWG = 256, NTHR = 512;
#define WINDOW 8
#define PRE 32
constexpr int WIN = WINDOW;
constexpr int NUNIT = 14;
constexpr int NM = 112;
constexpr int AUX_NT = 2, AUX_SC1 = 16;
#define AUX_K 2
#define VTOUCH 0
#define AUX_W 0
#define CODE_PF 1
#define F_SLEEP 24
#define RU0 32
#define LATE_I 6
#define LATE_AT 15
#define LEVEL 9
#define PACE2 0
#define PACE2_STEP 8
#define PACE2_OFF 100
#define PACE 0
#define TAU8 108
#define PACE_SLEEP 8
#define PACE_DELTA 40
constexpr unsigned OOB = 0x7FFFFFF0u;
constexpr float LOG2E = 1.4426950408889634f;

constexpr unsigned WS_EPOCH = 0, WS_HB = 16384, WS_Q = 1u << 17, WS_OX = 1u << 18, WS_ML = 1u << 19, WS_Y = 3u << 18,
                   WS_O = 7u << 18, WS_BYTES = 11u << 18;
constexpr unsigned WS_STAMP = 64u << 20;
constexpr int L_WO = 0;
constexpr int L_W1 = L_WO + 28672;
constexpr int L_W2 = L_W1 + 14336;
constexpr int L_SC = L_W2 + 14336;
constexpr int L_QB = L_SC + 8192;
constexpr int L_OA = L_QB + 512;
constexpr int L_RS = L_OA + 4096;
constexpr int L_HN = L_RS + 1024;
constexpr int HN_STRIDE = 912;
constexpr int L_MISC = L_HN + 4 * HN_STRIDE;
constexpr int L_ST = L_MISC + 256;
constexpr int L_BYTES = 84 * 1024;
static_assert(L_MISC + 1024 <= L_BYTES && L_MISC % 16 == 0 && L_HN % 16 == 0, "lds");
enum { M_ML = 0, M_WGT = 8, M_CNT_ML = 12, M_CNT_HN = 13, M_RDONE = 14, M_CNT_Q = 15, M_CNT_O = 16, M_WOK = 17, M_OPUB = 18, M_TAG = 19, M_LATE = 20, M_CNT_W = 21, M_MS = 22, M_EP = 24, M_PDONE = 26, M_CNT_R = 27, M_TS = 32 };

__device__ __forceinline__ float u2f(unsigned u) { return __builtin_bit_cast(float, u); }
__device__ __forceinline__ unsigned f2u(float f) { return __builtin_bit_cast(unsigned, f); }
__device__ __forceinline__ float rbf(float f) { return (float)(__bf16)f; }
__device__ __forceinline__ unsigned pk2(float lo, float hi) { bf16x2 v = {(__bf16)lo, (__bf16)hi}; return __builtin_bit_cast(unsigned, v); }
__device__ __forceinline__ float blo(unsigned u) { return u2f(u << 16); }
__device__ __forceinline__ float bhi(unsigned u) { return u2f(u & 0xffff0000u); }
__device__ __forceinline__ float dot2(unsigned a, unsigned b, float c) {
  return __builtin_amdgcn_fdot2_f32_bf16(__builtin_bit_cast(bf16x2, a), __builtin_bit_cast(bf16x2, b), c, false);
}
template <int CTRL> __device__ __forceinline__ float dpp(float x) {
  return __builtin_bit_cast(float, __builtin_amdgcn_mov_dpp(__builtin_bit_cast(int, x), CTRL, 0xf, 0xf, true));
}
constexpr int XOR1 = 0xB1, XOR2 = 0x4E, XOR7 = 0x141, ROR8 = 0x128, ROR4 = 0x124;
__device__ __forceinline__ float swap16_add(float a, float b) {
  auto s = __builtin_amdgcn_permlane16_swap(f2u(a), f2u(b), false, false);
  const unsigned s0 = s[0], s1 = s[1];
  return u2f(s0) + u2f(s1);
}
__device__ __forceinline__ float swap32_add(float a, float b) {
  auto s = __builtin_amdgcn_permlane32_swap(f2u(a), f2u(b), false, false);
  const unsigned s0 = s[0], s1 = s[1];
  return u2f(s0) + u2f(s1);
}
__device__ __forceinline__ float swap16_max(float a) {
  auto s = __builtin_amdgcn_permlane16_swap(f2u(a), f2u(a), false, false);
  const unsigned s0 = s[0], s1 = s[1];
  return fmaxf(u2f(s0), u2f(s1));
}
__device__ __forceinline__ float swap32_max(float a) {
  auto s = __builtin_amdgcn_permlane32_swap(f2u(a), f2u(a), false, false);
  const unsigned s0 = s[0], s1 = s[1];
  return fmaxf(u2f(s0), u2f(s1));
}
__device__ __forceinline__ float row_sum(float x) { x += dpp<ROR8>(x); x += dpp<ROR4>(x); x += dpp<XOR2>(x); x += dpp<XOR1>(x); return x; }
__device__ __forceinline__ float row_max(float x) { x = fmaxf(x, dpp<ROR8>(x)); x = fmaxf(x, dpp<ROR4>(x)); x = fmaxf(x, dpp<XOR2>(x)); x = fmaxf(x, dpp<XOR1>(x)); return x; }
__device__ __forceinline__ float wave_sum(float x) { x = row_sum(x); x = swap16_add(x, x); return swap32_add(x, x); }
__device__ __forceinline__ float wave_max(float x) { x = row_max(x); x = swap16_max(x); return swap32_max(x); }

__device__ __forceinline__ __amdgpu_buffer_rsrc_t mkrs(const void* p, unsigned bytes) { return __builtin_amdgcn_make_buffer_rsrc((void*)p, 0, bytes, 0x00020000); }
#define LD128(rs, vo, so, aux) __builtin_amdgcn_raw_buffer_load_b128(rs, vo, so, aux)
#define LD64(rs, vo, so, aux) __builtin_amdgcn_raw_buffer_load_b64(rs, vo, so, aux)
#define ST128(v, rs, vo, so, aux) __builtin_amdgcn_raw_buffer_store_b128(v, rs, vo, so, aux)
#define ST64(v, rs, vo, so, aux) __builtin_amdgcn_raw_buffer_store_b64(v, rs, vo, so, aux)
#define ST32(v, rs, vo, so, aux) __builtin_amdgcn_raw_buffer_store_b32(v, rs, vo, so, aux)
#define PIN() asm volatile("" ::: "memory")
#define USE(x) asm volatile("" :: "v"(x) : "memory")
#define REDEF(x) asm volatile("" : "+v"(x) :: "memory")
#define BARRIER() asm volatile("s_waitcnt lgkmcnt(0)\n\ts_barrier" ::: "memory")
#define DRAIN() asm volatile("s_waitcnt vmcnt(0)" ::: "memory")
#define LGKM0() asm volatile("s_waitcnt lgkmcnt(0)" ::: "memory")
constexpr int SPIN_CAP = 1 << 16;
constexpr int LSPIN_CAP = 1 << 20;

#define STAMP(i) do { if (lane == 0) *(volatile LAS u64*)(smem + L_ST + wave * 64 + (i) * 8) = __builtin_amdgcn_s_memrealtime(); } while (0)
#define STAMPV(i, v) do { if (lane == 0) *(volatile LAS u64*)(smem + L_ST + wave * 64 + (i) * 8) = (v); } while (0)
#define RET() do { LGKM0(); if (lane < 8) *(u64*)(ws + WS_STAMP + (size_t)g * 512 + wave * 64 + lane * 8) = *(volatile LAS u64*)(smem + L_ST + wave * 64 + lane * 8); return; } while (0)

__device__ __forceinline__ void ln8(float (&v)[8], int lane) {
  float s = ((v[0] + v[1]) + (v[2] + v[3])) + ((v[4] + v[5]) + (v[6] + v[7]));
  const float mean = wave_sum(s) * (1.0f / DM);
  float q = 0.f;
#pragma unroll
  for (int e = 0; e < 8; ++e) { const float d = v[e] - mean; q = fmaf(d, d, q); }
  q = lane < 56 ? q : 0.f;
  const float var = wave_sum(q) * (1.0f / DM);
  const float mb = rbf(mean);
  const float den = rbf(__builtin_amdgcn_sqrtf(rbf(rbf(var) + 1e-5f)));
  const float inv = __builtin_amdgcn_rcpf(den);
#pragma unroll
  for (int e = 0; e < 8; ++e) v[e] = rbf(v[e] - mb) * inv;
}
__device__ __forceinline__ float reduce_pieces(f32x4 v) {
  const float a = swap32_add(v[0], v[2]), b = swap32_add(v[1], v[3]);
  float c = swap16_add(a, b);
  c += dpp<ROR8>(c); c += dpp<ROR4>(c); c += dpp<XOR2>(c);
  return c;
}
__device__ __forceinline__ v4u enc4(f32x4 v, unsigned tag) {
  v4u r;
  r[0] = __builtin_amdgcn_perm(f2u(v[0]), tag, 0x07060500u); r[1] = __builtin_amdgcn_perm(f2u(v[1]), tag, 0x07060501u);
  r[2] = __builtin_amdgcn_perm(f2u(v[2]), tag, 0x07060502u); r[3] = __builtin_amdgcn_perm(f2u(v[3]), tag, 0x07060503u);
  return r;
}
__device__ __forceinline__ bool valid4(v4u a, unsigned tag) {
  return (__builtin_amdgcn_perm(a[1], a[0], 0x0c0c0400u) | __builtin_amdgcn_perm(a[3], a[2], 0x04000c0cu)) == tag;
}
__device__ __forceinline__ f32x4 dec4(v4u a) { return __builtin_bit_cast(f32x4, (a & 0xFFFFFF00u) | 0x80u); }
__device__ __forceinline__ f32x4 dec4x(v4u a) { return __builtin_bit_cast(f32x4, a & 0xFFFFFF00u); }

#define TS(i) do {} while (0)
#define SPIN(cap) _Pragma("unroll 1") for (int spin = 0; spin < (cap); ++spin)
#define LDS_INC(w) __hip_atomic_fetch_add((LAS unsigned*)(smem + L_MISC) + (w), 1u, __ATOMIC_RELAXED, __HIP_MEMORY_SCOPE_WORKGROUP)
#define LDS_WAIT_GE(w, n, slp) do { SPIN(LSPIN_CAP) { PIN(); if (misc[w] >= (unsigned)(n)) break; if (slp) __builtin_amdgcn_s_sleep(slp); } PIN(); } while (0)
template <int CTRL> __device__ __forceinline__ float comb(bool bit, float lo, float hi) {
  const float a = lo + dpp<CTRL>(lo), b = hi + dpp<CTRL>(hi);
  return bit ? b : a;
}
__device__ __forceinline__ f32x4 sel4(bool s1, bool s2, f32x4 a, f32x4 b, f32x4 c, f32x4 d) {
  f32x4 r;
#pragma unroll
  for (int k = 0; k < 4; ++k) { const float lo = s1 ? b[k] : a[k], hi = s1 ? d[k] : c[k]; r[k] = s2 ? hi : lo; }
  return r;
}
__device__ __forceinline__ bf16x4 trd(const LAS unsigned char* p) { return __builtin_amdgcn_ds_read_tr16_b64_v4bf16((LAS bf16x4*)p); }
__device__ __forceinline__ unsigned now32() { return (unsigned)__builtin_amdgcn_s_memrealtime(); }

__global__ void __launch_bounds__(NTHR, 2)
fused_decode(const u16* x, const u16* Kc, const u16* Vc, const u16* wq, const u16* wo, const u16* w1, const u16* w2, u16* out, unsigned char* ws) {
  __shared__ __attribute__((aligned(16))) unsigned char smem[L_BYTES];
  const int tid = threadIdx.x, lane = tid & 63;
  int wave = __builtin_amdgcn_readfirstlane(tid >> 6);
  const int g = blockIdx.x;
  const int b = g >> 6, h = (g >> 2) & 15, sp = g & 3;
  const int half = lane >> 5, c32 = lane & 31, g4 = lane >> 4, c16 = lane & 15;
  volatile LAS unsigned* misc = (volatile LAS unsigned*)(smem + L_MISC);
  const __amdgpu_buffer_rsrc_t rws = mkrs(ws, WS_BYTES);
  const __amdgpu_buffer_rsrc_t rk = mkrs(Kc + (size_t)g * (2048 * DH), 2048 * DH * 2);
  const __amdgpu_buffer_rsrc_t rx = mkrs(x, NB * DM * 2);
  const int voff = tid * 16;
  const int hw = wave - 4;
  v4u kb[PRE];
  u64 pc0;
  asm volatile("s_getpc_b64 %0\n.Lfd_code_begin:" : "=s"(pc0));
  unsigned clen, clen2;
  asm volatile("s_mov_b32 %0, .Lfd_code_end-.Lfd_code_begin\n\ts_mov_b32 %1, .Lfd_code_end2-.Lfd_code_begin" : "=s"(clen), "=s"(clen2));
  clen = clen > clen2 ? clen : clen2;
  const unsigned tstart = now32() + PACE2_OFF;
  if (lane < 8) *(volatile LAS u64*)(smem + L_ST + wave * 64 + lane * 8) = 0;
  STAMP(0);
  if (wave == 0) STAMPV(7, 1 + ((unsigned)__builtin_amdgcn_s_getreg(20 | (3 << 11)) & 0xfu));
  if (wave < 4) {
#pragma unroll
    for (int i = 0; i < WIN; ++i) kb[i] = LD128(rk, voff, i * 4096, AUX_K);
#pragma unroll
    for (int i = WIN; i < 12; ++i) asm volatile("" : "=v"(kb[i]));
  } else {
    const __amdgpu_buffer_rsrc_t rq = mkrs(wq + h * DH + (sp * 4 + b) * 16, LEVEL < 1 ? 0 : (DM - 1) * (NH * DH * 2) + 32);
    kb[0] = LD128(rx, lane < 56 ? lane * 16 : (int)OOB, hw * (DM * 2), 0);
#pragma unroll
    for (int i = 0; i < 4; ++i) kb[1 + i] = LD128(rq, (lane >> 1) * (NH * DH * 2) + (lane & 1) * 16, (hw + 4 * i) * 32 * (NH * DH * 2), AUX_W);
    kb[5] = LD128(rws, 0, WS_EPOCH, 0);
    {
      const __amdgpu_buffer_rsrc_t rc = mkrs((const void*)pc0, clen);
#pragma unroll
      for (int i = 0; i < 6; ++i) kb[6 + i] = LD128(rc, voff - 4096, i * 4096, 0);
    }
#pragma unroll
    for (int i = 12; i < WIN; ++i) asm volatile("" : "=v"(kb[i]));
    if (wave == 4 && lane >= 12 && lane < 32) misc[lane] = 0u;
  }
  BARRIER();
  asm volatile("" : "+s"(wave));

  const int unit = g & 63;
  const int oct = (g >> 7) * 16 + h, pj = (b & 1) * 4 + sp;
  if (wave < 4) {
    const __amdgpu_buffer_rsrc_t rv = mkrs(Vc + (size_t)g * (2048 * DH), 2048 * DH * 2);
#pragma unroll
    for (int i = WIN; i < PRE; ++i) { USE(kb[i - WIN]); kb[i] = LD128(rk, voff, i * 4096, AUX_K); PIN(); }
    STAMP(1);
    BARRIER();
    STAMP(2);
    const u64 c0_ = __builtin_amdgcn_s_memtime();
    const v4u qv = *(const v4u*)(smem + L_QB + c32 * 16);
    const unsigned tag = __builtin_amdgcn_readfirstlane(misc[M_TAG]);
    float* sc = (float*)(smem + L_SC) + (wave * 2 + half) * 256;
    float* scw = sc + (((c32 >> 4) & 1) | ((c32 >> 2) & 2) | (c32 & 4) | ((c32 << 2) & 8));
    const bool b3 = c32 & 8, b2 = c32 & 4, b1 = c32 & 2;
    float m = -INFINITY;
    float t0_, t1_, t2_, t3_;
#define COMB(CTRL, BIT, lo, hi) comb<CTRL>(BIT, lo, hi)
#define KSTEP(v, i, grp) do { \
      float d_ = dot2((v)[0], qv[0], 0.f); d_ = dot2((v)[1], qv[1], d_); d_ = dot2((v)[2], qv[2], d_); d_ = dot2((v)[3], qv[3], d_); \
      if (((i) & 1) == 0) t0_ = d_; else { d_ = swap16_add(t0_, d_); \
        if (((i) & 2) == 0) t1_ = d_; else { d_ = COMB(ROR8, b3, t1_, d_); \
          if (((i) & 4) == 0) t2_ = d_; else { d_ = COMB(XOR7, b2, t2_, d_); \
            if (((i) & 8) == 0) t3_ = d_; else { d_ = COMB(XOR2, b1, t3_, d_); d_ += dpp<XOR1>(d_); \
              d_ = rbf(d_) * 0.0625f; m = fmaxf(m, d_); scw[(grp) * 16] = d_; } } } } \
      asm volatile("" : "+v"(t0_), "+v"(t1_), "+v"(t2_), "+v"(t3_), "+v"(m)); } while (0)
#define PACE2_AT(row) do {} while (0)
    asm volatile("" : "=v"(t0_), "=v"(t1_), "=v"(t2_), "=v"(t3_));
#pragma unroll
    for (int i = 0; i < PRE; ++i) {
      REDEF(kb[i]);
      KSTEP(kb[i], i & 15, i >> 4);
      if (i < WIN) { PIN(); kb[i] = LD128(rk, voff, (PRE + i) * 4096, AUX_K); PIN(); }
    }
    TS(0);
#pragma unroll 1
    for (int gi = PRE / 16; gi < 16; ++gi) {
      const __amdgpu_buffer_rsrc_t rn = gi < 15 ? rk : rv;
      const int offn = gi < 15 ? (gi + 1) * 16 * 4096 : 0;
#pragma unroll
      for (int i = 0; i < 16; ++i) {
        if ((i & (PACE2_STEP - 1)) == 0) PACE2_AT(gi * 16 + i);
        USE(kb[i]);
        if (i + WIN < 16) kb[i + WIN] = LD128(rk, voff, (gi * 16 + i + WIN) * 4096, AUX_K);
        else kb[i + WIN - 16] = LD128(rn, voff, offn + (i + WIN - 16) * 4096, AUX_K);
        REDEF(kb[i]);
        KSTEP(kb[i], i, gi);
      }
      if (gi & 1) TS(gi >> 1);
    }
    STAMP(3);
    m = wave_max(m);
    {
      LGKM0();
      f32x4* ps = (f32x4*)(smem + L_SC) + wave * 128 + lane * 2;
      f32x4 a = ps[0], bq = ps[1];
      const float mm = m * LOG2E;
#pragma unroll
      for (int e = 0; e < 4; ++e) { a[e] = __builtin_amdgcn_exp2f(fmaf(a[e], LOG2E, -mm)); bq[e] = __builtin_amdgcn_exp2f(fmaf(bq[e], LOG2E, -mm)); }
      ps[0] = a; ps[1] = bq;
      float l = ((a[0] + a[1]) + (a[2] + a[3])) + ((bq[0] + bq[1]) + (bq[2] + bq[3]));
      l = wave_sum(l);
      if (lane == 0) { misc[M_ML + 2 * wave] = f2u(m); misc[M_ML + 2 * wave + 1] = f2u(l); }
      LGKM0();
      if (lane == 0) LDS_INC(M_CNT_ML);
    }
    float acc[8], wgt = 0.f;
#pragma unroll
    for (int k = 0; k < 8; ++k) acc[k] = 0.f;
#define VBODY(gi, LAST) do { \
      f32x4 p4[4]; \
      _Pragma("unroll") for (int i = 0; i < 4; ++i) p4[i] = *(const f32x4*)(sc + (gi) * 16 + i * 4); \
      TS(8 + (gi)); \
      _Pragma("unroll") for (int i = 0; i < 16; ++i) { \
        if ((i & (PACE2_STEP - 1)) == 0) PACE2_AT(256 + (gi) * 16 + i); \
        if ((LAST) && i == LATE_I && tid == 0) misc[M_LATE] = 1u; \
        USE(kb[i]); \
        if (!(LAST) || i + WIN < 16) kb[(i + WIN) & 15] = LD128(rv, voff, ((gi) * 16 + i + WIN) * 4096, AUX_NT); \
        REDEF(kb[i]); \
        const float p = p4[i >> 2][i & 3]; \
        _Pragma("unroll") for (int k = 0; k < 4; ++k) { acc[2 * k] = fmaf(p, blo(kb[i][k]), acc[2 * k]); acc[2 * k + 1] = fmaf(p, bhi(kb[i][k]), acc[2 * k + 1]); } \
        asm volatile("" : "+v"(acc[0]), "+v"(acc[1]), "+v"(acc[2]), "+v"(acc[3]), "+v"(acc[4]), "+v"(acc[5]), "+v"(acc[6]), "+v"(acc[7])); \
      } } while (0)
#pragma unroll 1
    for (int gi = 0; gi < 15; ++gi) VBODY(gi, false);
    LDS_WAIT_GE(M_WOK, 1, 2);
    wgt = u2f(misc[M_WGT + wave]);
    VBODY(15, true);
    STAMP(4);
    if (wave == 2) STAMPV(7, __builtin_amdgcn_s_memtime() - c0_);
    {
      f32x4 o;
#pragma unroll
      for (int k = 0; k < 4; ++k) o[k] = swap32_add(acc[k], acc[k + 4]) * wgt;
      f32x4* oa = (f32x4*)(smem + L_OA) + lane;
      oa[wave * 64] = o;
      LGKM0();
      unsigned old = 0;
      if (lane == 0) old = LDS_INC(M_CNT_O);
      if (__builtin_amdgcn_readfirstlane(old) == 3u) {
        PIN();
        o = (oa[0] + oa[64]) + (oa[128] + oa[192]);
        ST128(enc4(o, tag), rws, (c32 >> 2) * 1024 + (c32 & 3) * 32 + half * 16, WS_OX + oct * 8192 + pj * 128, AUX_SC1);
      }
    }
    STAMP(5);
    const int ru = unit - RU0;
    if (ru < 0 || ru >= NUNIT) RET();
    const v2u xr = LD64(rx, (lane & 7) * 8, b * (DM * 2) + ru * 64, 0);
    const int po = (lane >> 3) * (DM * 4) + (lane & 7) * 16;
    float* rs = (float*)(smem + L_RS);
    LDS_WAIT_GE(M_PDONE, 1, 1);
#pragma unroll 1
    for (int ph = 0; ph < 2; ++ph) {
      const unsigned ib = (ph == 0 ? WS_Y + (unsigned)(b * 128 + wave * 32) * (DM * 4u) : WS_O + (unsigned)(b * NM + wave * 28) * (DM * 4u)) + ru * 128;
      v4u p0 = {0u, 0u, 0u, 0u}, p1 = p0, p2 = p0, p3 = p0;
      bool k0 = false, k1 = false, k2 = false, k3 = ph == 1 && lane >= 32;
      SPIN(SPIN_CAP) {
        PIN();
        const v4u n0 = LD128(rws, k0 ? (int)OOB : po, ib, AUX_SC1), n1 = LD128(rws, k1 ? (int)OOB : po, ib + 8 * DM * 4, AUX_SC1);
        const v4u n2 = LD128(rws, k2 ? (int)OOB : po, ib + 16 * DM * 4, AUX_SC1), n3 = LD128(rws, k3 ? (int)OOB : po, ib + 24 * DM * 4, AUX_SC1);
        p0 = k0 ? p0 : n0; p1 = k1 ? p1 : n1; p2 = k2 ? p2 : n2; p3 = k3 ? p3 : n3;
        k0 = valid4(p0, tag); k1 = valid4(p1, tag); k2 = valid4(p2, tag); k3 |= valid4(p3, tag);
        if (__all(k0 & k1 & k2 & k3)) break;
      }
      if (ph == 0 || (wave & 1)) STAMP(6 + ph);
      const f32x4 v = (dec4(p0) + dec4(p1)) + (dec4(p2) + dec4(p3));
      float c = swap16_add(swap32_add(v[0], v[2]), swap32_add(v[1], v[3]));
      c += dpp<ROR8>(c);
      rs[wave * 32 + (lane & 7) * 4 + 2 * half + (g4 & 1)] = c;
      LGKM0();
      unsigned old = 0;
      if (lane == 0) old = LDS_INC(M_CNT_R);
      if (__builtin_amdgcn_readfirstlane(old) == 3u + 4u * ph) {
        PIN();
        const f32x4* r4 = (const f32x4*)rs + (lane & 7);
        const f32x4 sum = (r4[0] + r4[8]) + (r4[16] + r4[24]);
        f32x4 res;
        if (ph == 0) {
          res[0] = rbf(blo(xr[0]) + rbf(sum[0])); res[1] = rbf(bhi(xr[0]) + rbf(sum[1])); res[2] = rbf(blo(xr[1]) + rbf(sum[2])); res[3] = rbf(bhi(xr[1]) + rbf(sum[3]));
          ST128(enc4(res, tag), rws, (lane >> 3) * (NB * DM * 4) + (lane & 7) * 16, WS_HB + b * (DM * 4) + ru * 128, AUX_SC1);
          *((f32x4*)rs + 32 + (lane & 7)) = res;
          LGKM0();
          if (lane == 0) misc[M_RDONE] = 1u;
        } else {
          const f32x4 hq = r4[32];
          const v2u ov = {pk2(hq[0] + rbf(sum[0]), hq[1] + rbf(sum[1])), pk2(hq[2] + rbf(sum[2]), hq[3] + rbf(sum[3]))};
          ST64(ov, mkrs(out, NB * DM * 2), lane < 8 ? lane * 8 : (int)OOB, b * (DM * 2) + ru * 64, 0);
          if (g == RU0) {
            const v2u ne = {misc[M_EP], misc[M_EP + 1]};
            ST64(ne, rws, lane == 0 ? 0 : (int)OOB, WS_EPOCH, AUX_SC1);
            {
              const u64 tn = __builtin_amdgcn_s_memrealtime();
              const u64 ts = *(volatile LAS u64*)(smem + L_ST);
              const u64 tp = PROBE == 0 ? ts : PROBE == 9 ? tn : *(volatile LAS u64*)(smem + L_ST + (PROBE >> 4) * 64 + (PROBE & 15) * 8);
              const u64 target = tp + 20000;
              SPIN(1 << 20) { if (__builtin_amdgcn_s_memrealtime() >= target) break; __builtin_amdgcn_s_sleep(2); }
            }
          }
        }
      }
      if (ph == 0) LDS_WAIT_GE(M_RDONE, 1, 1);
    }
    STAMP(1);
    asm volatile(".Lfd_code_end2:");
    RET();
  }
  const bool is_m = unit < 28;
  const int mi = unit * 4 + b;
  const int tq = c16 >> 2, tp = c16 & 3;
  const LAS unsigned char* s1 = (const LAS unsigned char*)(smem + L_W1) + (g4 * 8 + tq) * 32 + tp * 8;
  const unsigned char* hp = smem + L_HN + (c16 & 3) * HN_STRIDE + g4 * 16;
  {
    float v[8];
#pragma unroll
    for (int k = 0; k < 4; ++k) { v[2 * k] = blo(kb[0][k]); v[2 * k + 1] = bhi(kb[0][k]); }
    ln8(v, lane);
    const v4u xn = {pk2(v[0], v[1]), pk2(v[2], v[3]), pk2(v[4], v[5]), pk2(v[6], v[7])};
    if (lane < 56) *(v4u*)(smem + L_HN + hw * HN_STRIDE + lane * 16) = xn;
#pragma unroll
    for (int i = 0; i < 4; ++i) if (hw + 4 * i < 14) *(v4u*)(smem + L_W1 + (hw + 4 * i) * 1024 + lane * 16) = kb[1 + i];
    LGKM0();
    if (lane == 0) LDS_INC(M_CNT_Q);
  }
  const unsigned e0 = __builtin_amdgcn_readfirstlane(kb[5][0]), e1 = __builtin_amdgcn_readfirstlane(kb[5][1]);
  unsigned qtag = e0 + 0x9E3779B9u;
  qtag ^= qtag >> 16; qtag *= 0x85EBCA6Bu; qtag ^= qtag >> 13; qtag *= 0xC2B2AE35u; qtag ^= qtag >> 16;
  if (hw == 0) {
    LDS_WAIT_GE(M_CNT_Q, 4, 0);
    bf16x8 a1[14], bfr[14];
#pragma unroll
    for (int s = 0; s < 14; ++s) {
      a1[s] = __builtin_shufflevector(trd(s1 + s * 1024), trd(s1 + s * 1024 + 128), 0, 1, 2, 3, 4, 5, 6, 7);
      bfr[s] = *(const bf16x8*)(hp + s * 64);
    }
    __builtin_amdgcn_sched_barrier(0);
    f32x4 da = {0.f, 0.f, 0.f, 0.f}, db = {0.f, 0.f, 0.f, 0.f};
#pragma unroll
    for (int s = 0; s < 14; s += 2) {
      da = __builtin_amdgcn_mfma_f32_16x16x32_bf16(a1[s], bfr[s], da, 0, 0, 0);
      db = __builtin_amdgcn_mfma_f32_16x16x32_bf16(a1[s + 1], bfr[s + 1], db, 0, 0, 0);
    }
    da += db;
    const v4u qg = {pk2(da[0], da[1]), qtag, pk2(da[2], da[3]), qtag};
    ST128(qg, rws, c16 < 4 ? c16 * 64 + g4 * 16 : (int)OOB, WS_Q + h * 4096 + (sp * 4 + b) * 256, AUX_SC1);
    if (lane == 0) LDS_INC(M_CNT_Q);
    STAMP(1);
  } else if (hw == 3) {
    if (lane == 0) { misc[M_TAG] = qtag; misc[M_EP] = e0 + 1u; misc[M_EP + 1] = e1; }
  } else if (hw == 1) {
    v4u qg;
    LDS_WAIT_GE(M_CNT_Q, 5, 1);
    SPIN(SPIN_CAP) {
      PIN();
      qg = LD128(rws, (lane >> 2) * 256 + (lane & 3) * 16, WS_Q + h * 4096 + b * 64, AUX_SC1);
      if (__all(qg[1] == qtag && qg[3] == qtag)) break;
    }
    *(v2u*)(smem + L_QB + lane * 8) = v2u{qg[0], qg[2]};
    STAMP(1);
  }
#pragma unroll
  for (int i = 6; i < 12; ++i) USE(kb[i]);
  BARRIER();
  bf16x8 a0[8];
  {
    const __amdgpu_buffer_rsrc_t r0 = mkrs(wo + (size_t)(h * DH + pj * 32) * DM, 32 * DM * 2);
    v4u t0[7];
#pragma unroll
    for (int i = 0; i < 7; ++i) t0[i] = LD128(r0, lane * 16, (hw + 4 * i) * 1024, AUX_W);
    const unsigned d1 = __builtin_amdgcn_raw_buffer_load_b16(mkrs(hw == 0 ? w1 : hw == 1 ? w2 : hw == 2 ? w1 + DM * FF - 64 : w2 + DM * FF - 64, 64), 0, 0, 0);
#pragma unroll
    for (int i = 0; i < 7; ++i) *(v4u*)(smem + L_WO + (hw + 4 * i) * 1024 + lane * 16) = t0[i];
    USE(d1);
    LGKM0();
    if (lane == 0) LDS_INC(M_CNT_W);
    LDS_WAIT_GE(M_CNT_W, 4, 2);
    const LAS unsigned char* s0 = (const LAS unsigned char*)(smem + L_WO) + (g4 * 8 + tq) * (DM * 2) + (hw * 64 + tp * 4) * 2;
#pragma unroll
    for (int t = 0; t < 8; ++t) a0[t] = __builtin_shufflevector(trd(s0 + (t & 3) * 32 + (t >> 2) * 512), trd(s0 + (t & 3) * 32 + (t >> 2) * 512 + 4 * DM * 2), 0, 1, 2, 3, 4, 5, 6, 7);
#pragma unroll
    for (int t = 0; t < 8; ++t) asm volatile("" : "+v"(a0[t]));
  }
  if (hw == 0) {
    LDS_WAIT_GE(M_CNT_ML, 4, 16);
    const float mo = u2f(misc[M_ML + 2 * (lane & 3)]), lo = u2f(misc[M_ML + 2 * (lane & 3) + 1]);
    float M = fmaxf(mo, dpp<XOR1>(mo)); M = fmaxf(M, dpp<XOR2>(M));
    const float e = __builtin_amdgcn_exp2f((mo - M) * LOG2E);
    float L = lo * e; L += dpp<XOR1>(L); L += dpp<XOR2>(L);
    if (lane < 4) misc[M_WGT + lane] = f2u(e);
    if (lane == 0) { misc[M_MS] = f2u(M); misc[M_MS + 1] = f2u(L); }
    LGKM0();
    if (lane == 0) misc[M_WOK] = 1u;
    const unsigned dummy = __builtin_amdgcn_raw_buffer_load_b16(mkrs(out, NB * DM * 2), 0, 0, 0);
    USE(dummy);
  }
  STAMP(4);
  LDS_WAIT_GE(M_LATE, 1, 4);
  if (hw == 0) {
    const f32x4 mlv = {u2f(misc[M_MS]), u2f(misc[M_MS + 1]), 0.f, 0.f};
    ST128(enc4(mlv, qtag), rws, lane < 8 ? lane * 1024 : (int)OOB, WS_ML + oct * 8192 + pj * 128, AUX_SC1);
  }
  v4u t1[4], t2[4];
  {
    const __amdgpu_buffer_rsrc_t r1 = mkrs(w1 + mi * 16, is_m ? (DM - 1) * (FF * 2) + 32 : 0);
    const __amdgpu_buffer_rsrc_t r2 = mkrs(w2 + (size_t)mi * 16 * DM, is_m ? 16 * DM * 2 : 0);
#pragma unroll
    for (int i = 0; i < 4; ++i) {
      t1[i] = LD128(r1, (lane >> 1) * (FF * 2) + (lane & 1) * 16, (hw + 4 * i) * 32 * (FF * 2), AUX_W);
      t2[i] = LD128(r2, lane * 16, (hw + 4 * i) * 1024, AUX_W);
    }
    PIN();
  }
  STAMP(3);
  {
    v4u pv;
    SPIN(SPIN_CAP) {
      PIN();
      pv = LD128(rws, (c16 & 7) * 128, WS_ML + oct * 8192 + pj * 1024, AUX_SC1);
      if (__all(valid4(pv, qtag))) break;
      __builtin_amdgcn_s_sleep(1);
    }
    float wn;
    {
      const float ms = u2f(pv[0] & 0xFFFFFF00u), ls = u2f((pv[1] & 0xFFFFFF00u) | 0x80u);
      float M = fmaxf(ms, dpp<XOR1>(ms)); M = fmaxf(M, dpp<XOR2>(M));
      const float e = __builtin_amdgcn_exp2f((ms - M) * LOG2E);
      float L = ls * e; L += dpp<XOR1>(L); L += dpp<XOR2>(L);
      wn = e * __builtin_amdgcn_rcpf(L);
    }
    const int po = ((c16 >> 2) & 1) * 512 + (c16 & 3) * 128 + g4 * 32 + (c16 >> 3) * 16;
    SPIN(SPIN_CAP) {
      PIN();
      pv = LD128(rws, po, WS_OX + oct * 8192 + pj * 1024, AUX_SC1);
      if (__all(valid4(pv, qtag))) break;
    }
    STAMP(5);
    f32x4 o = dec4(pv);
#pragma unroll
    for (int k = 0; k < 4; ++k) { o[k] *= wn; o[k] += dpp<XOR1>(o[k]); o[k] += dpp<XOR2>(o[k]); }
    v4u bo;
    bo[0] = pk2(o[0], o[1]); bo[1] = pk2(o[2], o[3]);
    bo[2] = f2u(dpp<ROR8>(u2f(bo[0]))); bo[3] = f2u(dpp<ROR8>(u2f(bo[1])));
    f32x4 y[8];
#pragma unroll
    for (int t = 0; t < 8; ++t) y[t] = __builtin_amdgcn_mfma_f32_16x16x32_bf16(a0[t], __builtin_bit_cast(bf16x8, bo), f32x4{0.f, 0.f, 0.f, 0.f}, 0, 0, 0);
    __builtin_amdgcn_sched_barrier(0);
    const int vo = c16 < 8 ? (c16 >> 2) * (128 * DM * 4) + (c16 & 3) * 64 + g4 * 16 : (int)OOB;
    const bool s1b = c16 & 1, s2b = c16 & 2;
    const unsigned yb = WS_Y + (unsigned)((b & 2) * 128 + h * 8 + pj) * (DM * 4u) + hw * 256;
    ST128(enc4(sel4(s1b, s2b, y[0], y[1], y[2], y[3]), qtag), rws, vo, yb, AUX_SC1);
    ST128(enc4(sel4(s1b, s2b, y[4], y[5], y[6], y[7]), qtag), rws, hw < 3 ? vo : (int)OOB, yb + 1024, AUX_SC1);
    if (lane == 0) misc[M_PDONE] = 1u;
  }
  STAMP(6);
  if (!is_m) RET();
  bf16x8 a1[14];
  s16x4 a2[8];
  {
#pragma unroll
    for (int i = 0; i < 4; ++i) if (hw + 4 * i < 14) {
      *(v4u*)(smem + L_W1 + (hw + 4 * i) * 1024 + lane * 16) = t1[i];
      *(v4u*)(smem + L_W2 + (hw + 4 * i) * 1024 + lane * 16) = t2[i];
    }
    LGKM0();
    if (lane == 0) LDS_INC(M_CNT_W);
    LDS_WAIT_GE(M_CNT_W, 8, 0);
    const LAS unsigned char* s2 = (const LAS unsigned char*)(smem + L_W2) + (g4 * 4 + tq) * (DM * 2) + (hw * 64 + tp * 4) * 2;
#pragma unroll
    for (int t = 0; t < 8; ++t) a2[t] = __builtin_bit_cast(s16x4, trd(s2 + (t & 3) * 32 + (t >> 2) * 512));
    asm volatile("" : "+v"(a2[7]));
#pragma unroll
    for (int s = 0; s < 14; ++s) a1[s] = __builtin_shufflevector(trd(s1 + s * 1024), trd(s1 + s * 1024 + 128), 0, 1, 2, 3, 4, 5, 6, 7);
#pragma unroll
    for (int t = 0; t < 7; ++t) asm volatile("" : "+v"(a2[t]), "+v"(a1[2 * t]), "+v"(a1[2 * t + 1]));
  }
  v4u h0, h1;
  {
    const unsigned hb = WS_HB + (unsigned)(g & 7) * (NB * DM * 4) + hw * (DM * 4);
    const int vo = lane < 56 ? lane * 32 : (int)OOB;
    SPIN(SPIN_CAP) {
      PIN();
      h0 = LD128(rws, vo, hb, AUX_SC1); h1 = LD128(rws, vo, hb + 16, AUX_SC1);
      const bool ok = (valid4(h0, qtag) & valid4(h1, qtag)) | (lane >= 56);
      if (__all(ok)) break;
    }
  }
  STAMP(7);
  {
    float v[8];
#pragma unroll
    for (int k = 0; k < 4; ++k) { v[k] = bhi(h0[k]); v[k + 4] = bhi(h1[k]); }
    ln8(v, lane);
    const v4u hn = {pk2(v[0], v[1]), pk2(v[2], v[3]), pk2(v[4], v[5]), pk2(v[6], v[7])};
    if (lane < 56) *(v4u*)(smem + L_HN + hw * HN_STRIDE + lane * 16) = hn;
    LGKM0();
    if (lane == 0) LDS_INC(M_CNT_HN);
    LDS_WAIT_GE(M_CNT_HN, 4, 0);
  }
  f32x4 d1a = {0.f, 0.f, 0.f, 0.f}, d1b = {0.f, 0.f, 0.f, 0.f};
  {
    bf16x8 bfr[14];
#pragma unroll
    for (int s = 0; s < 14; ++s) bfr[s] = *(const bf16x8*)(hp + s * 64);
    __builtin_amdgcn_sched_barrier(0);
#pragma unroll
    for (int s = 0; s < 14; s += 2) {
      d1a = __builtin_amdgcn_mfma_f32_16x16x32_bf16(a1[s], bfr[s], d1a, 0, 0, 0);
      d1b = __builtin_amdgcn_mfma_f32_16x16x32_bf16(a1[s + 1], bfr[s + 1], d1b, 0, 0, 0);
    }
  }
  float sv[4];
#pragma unroll
  for (int r = 0; r < 4; ++r) { const float t = rbf(d1a[r] + d1b[r]); sv[r] = t * __builtin_amdgcn_rcpf(1.0f + __builtin_amdgcn_exp2f(-t * LOG2E)); }
  const v2u hbp = {pk2(sv[0], sv[1]), pk2(sv[2], sv[3])};
  const s16x4 b2v = __builtin_bit_cast(s16x4, hbp);
  const int vo = (c16 & 3) * (NM * DM * 4) + (c16 >> 2) * 64 + g4 * 16;
  f32x4 o2[8];
#pragma unroll
  for (int t = 0; t < 8; ++t) o2[t] = __builtin_amdgcn_mfma_f32_16x16x16bf16_1k(a2[t], b2v, f32x4{0.f, 0.f, 0.f, 0.f}, 0, 0, 0);
  __builtin_amdgcn_sched_barrier(0);
  {
    const bool s1b = c16 & 4, s2b = c16 & 8;
    const unsigned ob = WS_O + (unsigned)mi * (DM * 4u) + hw * 256;
    ST128(enc4(sel4(s1b, s2b, o2[0], o2[1], o2[2], o2[3]), qtag), rws, vo, ob, AUX_SC1);
    ST128(enc4(sel4(s1b, s2b, o2[4], o2[5], o2[6], o2[7]), qtag), rws, hw < 3 ? vo : (int)OOB, ob + 1024, AUX_SC1);
  }
  STAMP(2);
  asm volatile(".Lfd_code_end:");
  RET();
}

extern "C" void kernel_launch(void* const* d_in, const int* in_sizes, int n_in, void* d_out, int out_size, void* d_ws, size_t ws_size, hipStream_t stream) {
  fused_decode<<<NWG, NTHR, 0, stream>>>((const u16*)d_in[0], (const u16*)d_in[1], (const u16*)d_in[2], (const u16*)d_in[3], (const u16*)d_in[4],
                                         (const u16*)d_in[6], (const u16*)d_in[7], (u16*)d_out, (unsigned char*)d_ws);
}
```

```cpp
#include <hip/hip_runtime.h>
#include <stdint.h>

typedef unsigned v4u __attribute__((ext_vector_type(4)));
typedef unsigned v2u __attribute__((ext_vector_type(2)));
typedef float f32x4 __attribute__((ext_vector_type(4)));
typedef __bf16 bf16x2 __attribute__((ext_vector_type(2)));
typedef __bf16 bf16x4 __attribute__((ext_vector_type(4)));
typedef __bf16 bf16x8 __attribute__((ext_vector_type(8)));
typedef short s16x4 __attribute__((ext_vector_type(4)));
typedef unsigned short u16;
typedef unsigned long long u64;
#define LAS __attribute__((address_space(3)))

constexpr int NB = 4, DM = 448, NH = 16, SK = 8192, DH = 256, FF = 1792;
constexpr int NWG = 256, NTHR = 512;
#define WINDOW 8
#define PRE 32
constexpr int WIN = WINDOW;
constexpr int NUNIT = 14;
constexpr int NM = 112;
constexpr int AUX_NT = 2, AUX_SC1 = 16;
#define AUX_W 0
#define RU0 32
#define LATE_I 6
#define PACE2 7000
#define PACE2_STEP 4
#define PACE2_OFF 400
#define SXMODE 1
#define SX_STAGGER 0
#define AFFINE 1
#define GS 4
#define NPROBE 1
#define PROBE_K "3"
#define VOTE_THR 150
#define POOL_THR 28
constexpr unsigned OOB = 0x7FFFFFF0u;
constexpr float LOG2E = 1.4426950408889634f;
constexpr float SCL2E = 0.0625f * LOG2E;

constexpr unsigned WS_EPOCH = 0, WS_HB = 16384, WS_Q = 1u << 17, WS_OX = 1u << 18, WS_ML = 1u << 19, WS_Y = 3u << 18,
                   WS_O = 7u << 18, WS_SX = 3u << 20, WS_BYTES = 5u << 20;
constexpr unsigned WS_STAMP = 64u << 20;
constexpr int L_WO = 0;
constexpr int L_W1 = L_WO + 28672;
constexpr int L_W2 = L_W1 + 14336;
constexpr int L_SC = L_W2 + 14336;
constexpr int L_PV = L_SC + 8192;
constexpr int L_QB = L_PV + 8192;
constexpr int L_OA = L_QB + 512;
constexpr int L_RS = L_OA + 4096;
constexpr int L_HN = L_RS + 1024;
constexpr int HN_STRIDE = 912;
constexpr int L_MISC = L_HN + 4 * HN_STRIDE;
constexpr int L_ST = L_MISC + 256;
constexpr int L_BYTES = 88 * 1024;
static_assert(L_MISC + 1024 <= L_BYTES && L_MISC % 16 == 0 && L_HN % 16 == 0, "lds");
enum { M_PL = 0, M_MS = 4, M_ALPHA = 6, M_CNT_S = 8, M_P1 = 9, M_P2 = 10, M_DEC = 11, M_CNT_HN = 13, M_RDONE = 14, M_CNT_Q = 15, M_CNT_O = 16, M_TAG = 19, M_LATE = 20,
       M_CNT_W = 21, M_EP = 24, M_PDONE = 26, M_CNT_R = 27, M_TS = 32 };

__device__ __forceinline__ float u2f(unsigned u) { return __builtin_bit_cast(float, u); }
__device__ __forceinline__ unsigned f2u(float f) { return __builtin_bit_cast(unsigned, f); }
__device__ __forceinline__ float rbf(float f) { return (float)(__bf16)f; }
__device__ __forceinline__ unsigned pk2(float lo, float hi) { bf16x2 v = {(__bf16)lo, (__bf16)hi}; return __builtin_bit_cast(unsigned, v); }
__device__ __forceinline__ float blo(unsigned u) { return u2f(u << 16); }
__device__ __forceinline__ float bhi(unsigned u) { return u2f(u & 0xffff0000u); }
__device__ __forceinline__ float dot2(unsigned a, unsigned b, float c) {
  return __builtin_amdgcn_fdot2_f32_bf16(__builtin_bit_cast(bf16x2, a), __builtin_bit_cast(bf16x2, b), c, false);
}
template <int CTRL> __device__ __forceinline__ float dpp(float x) {
  return __builtin_bit_cast(float, __builtin_amdgcn_mov_dpp(__builtin_bit_cast(int, x), CTRL, 0xf, 0xf, true));
}
constexpr int XOR1 = 0xB1, XOR2 = 0x4E, XOR7 = 0x141, ROR8 = 0x128, ROR4 = 0x124;
__device__ __forceinline__ float swap16_add(float a, float b) {
  auto s = __builtin_amdgcn_permlane16_swap(f2u(a), f2u(b), false, false);
  const unsigned s0 = s[0], s1 = s[1];
  return u2f(s0) + u2f(s1);
}
__device__ __forceinline__ float swap32_add(float a, float b) {
  auto s = __builtin_amdgcn_permlane32_swap(f2u(a), f2u(b), false, false);
  const unsigned s0 = s[0], s1 = s[1];
  return u2f(s0) + u2f(s1);
}
__device__ __forceinline__ float swap16_max(float a) {
  auto s = __builtin_amdgcn_permlane16_swap(f2u(a), f2u(a), false, false);
  const unsigned s0 = s[0], s1 = s[1];
  return fmaxf(u2f(s0), u2f(s1));
}
__device__ __forceinline__ float swap32_max(float a) {
  auto s = __builtin_amdgcn_permlane32_swap(f2u(a), f2u(a), false, false);
  const unsigned s0 = s[0], s1 = s[1];
  return fmaxf(u2f(s0), u2f(s1));
}
__device__ __forceinline__ float row_sum(float x) { x += dpp<ROR8>(x); x += dpp<ROR4>(x); x += dpp<XOR2>(x); x += dpp<XOR1>(x); return x; }
__device__ __forceinline__ float row_max(float x) { x = fmaxf(x, dpp<ROR8>(x)); x = fmaxf(x, dpp<ROR4>(x)); x = fmaxf(x, dpp<XOR2>(x)); x = fmaxf(x, dpp<XOR1>(x)); return x; }
__device__ __forceinline__ float wave_sum(float x) { x = row_sum(x); x = swap16_add(x, x); return swap32_add(x, x); }
__device__ __forceinline__ float wave_max(float x) { x = row_max(x); x = swap16_max(x); return swap32_max(x); }

__device__ __forceinline__ __amdgpu_buffer_rsrc_t mkrs(const void* p, unsigned bytes) { return __builtin_amdgcn_make_buffer_rsrc((void*)p, 0, bytes, 0x00020000); }
#define LD128(rs, vo, so, aux) __builtin_amdgcn_raw_buffer_load_b128(rs, vo, so, aux)
#define LD64(rs, vo, so, aux) __builtin_amdgcn_raw_buffer_load_b64(rs, vo, so, aux)
#define ST128(v, rs, vo, so, aux) do { const v4u sv_ = (v); __builtin_amdgcn_raw_buffer_store_b128(sv_, rs, vo, so, aux); asm volatile("s_nop 1" :: "v"(sv_) : "memory"); } while (0)
#define ST64(v, rs, vo, so, aux) __builtin_amdgcn_raw_buffer_store_b64(v, rs, vo, so, aux)
#define ST32(v, rs, vo, so, aux) __builtin_amdgcn_raw_buffer_store_b32(v, rs, vo, so, aux)
#define PIN() asm volatile("" ::: "memory")
#define USE(x) asm volatile("" :: "v"(x) : "memory")
#define REDEF(x) asm volatile("" : "+v"(x) :: "memory")
#define BARRIER() asm volatile("s_waitcnt lgkmcnt(0)\n\ts_barrier" ::: "memory")
#define DRAIN() asm volatile("s_waitcnt vmcnt(0)" ::: "memory")
#define LGKM0() asm volatile("s_waitcnt lgkmcnt(0)" ::: "memory")
constexpr int SPIN_CAP = 1 << 16;
constexpr int LSPIN_CAP = 1 << 20;

#define STAMP(i) do {} while (0)
#define RET() return

__device__ __forceinline__ void ln8(float (&v)[8], int lane) {
  float s = ((v[0] + v[1]) + (v[2] + v[3])) + ((v[4] + v[5]) + (v[6] + v[7]));
  const float mean = wave_sum(s) * (1.0f / DM);
  float q = 0.f;
#pragma unroll
  for (int e = 0; e < 8; ++e) { const float d = v[e] - mean; q = fmaf(d, d, q); }
  q = lane < 56 ? q : 0.f;
  const float var = wave_sum(q) * (1.0f / DM);
  const float mb = rbf(mean);
  const float den = rbf(__builtin_amdgcn_sqrtf(rbf(rbf(var) + 1e-5f)));
  const float inv = __builtin_amdgcn_rcpf(den);
#pragma unroll
  for (int e = 0; e < 8; ++e) v[e] = rbf(v[e] - mb) * inv;
}
__device__ __forceinline__ v4u enc4(f32x4 v, unsigned tag) {
  v4u r;
  r[0] = __builtin_amdgcn_perm(f2u(v[0]), tag, 0x07060500u); r[1] = __builtin_amdgcn_perm(f2u(v[1]), tag, 0x07060501u);
  r[2] = __builtin_amdgcn_perm(f2u(v[2]), tag, 0x07060502u); r[3] = __builtin_amdgcn_perm(f2u(v[3]), tag, 0x07060503u);
  return r;
}
__device__ __forceinline__ bool valid4(v4u a, unsigned tag) {
  return (__builtin_amdgcn_perm(a[1], a[0], 0x0c0c0400u) | __builtin_amdgcn_perm(a[3], a[2], 0x04000c0cu)) == tag;
}
__device__ __forceinline__ f32x4 dec4(v4u a) { return __builtin_bit_cast(f32x4, (a & 0xFFFFFF00u) | 0x80u); }
__device__ __forceinline__ f32x4 dec4x(v4u a) { return __builtin_bit_cast(f32x4, a & 0xFFFFFF00u); }

#define TS(i) do {} while (0)
#define SPIN(cap) _Pragma("unroll 1") for (int spin = 0; spin < (cap); ++spin)
#define LDS_INC(w) __hip_atomic_fetch_add((LAS unsigned*)(smem + L_MISC) + (w), 1u, __ATOMIC_RELAXED, __HIP_MEMORY_SCOPE_WORKGROUP)
#define LDS_WAIT_GE(w, n, slp) do { SPIN(LSPIN_CAP) { PIN(); if (misc[w] >= (unsigned)(n)) break; if (slp) __builtin_amdgcn_s_sleep(slp); } PIN(); } while (0)
template <int CTRL> __device__ __forceinline__ float comb(bool bit, float lo, float hi) {
  const float a = lo + dpp<CTRL>(lo), b = hi + dpp<CTRL>(hi);
  return bit ? b : a;
}
__device__ __forceinline__ f32x4 sel4(bool s1, bool s2, f32x4 a, f32x4 b, f32x4 c, f32x4 d) {
  f32x4 r;
#pragma unroll
  for (int k = 0; k < 4; ++k) { const float lo = s1 ? b[k] : a[k], hi = s1 ? d[k] : c[k]; r[k] = s2 ? hi : lo; }
  return r;
}
__device__ __forceinline__ bf16x4 trd(const LAS unsigned char* p) { return __builtin_amdgcn_ds_read_tr16_b64_v4bf16((LAS bf16x4*)p); }
__device__ __forceinline__ unsigned now32() { return (unsigned)__builtin_amdgcn_s_memrealtime(); }

__global__ void __launch_bounds__(NTHR, 2)
fused_decode(const u16* x, const u16* Kc, const u16* Vc, const u16* wq, const u16* wo, const u16* w1, const u16* w2, u16* out, unsigned char* ws) {
  __shared__ __attribute__((aligned(16))) unsigned char smem[L_BYTES];
  const int tid = threadIdx.x, lane = tid & 63;
  int wave = __builtin_amdgcn_readfirstlane(tid >> 6);
  const int blk = blockIdx.x;
  const int b = blk >> 6, h = ((blk >> 3) & 7) * 2 + ((blk >> 1) & 1), side = (blk >> 2) & 1, sp = (blk & 1) * 2 + side;
  const int reg = (blk >> 3) * 4 + (blk & 3);
  const int half = lane >> 5, c32 = lane & 31, g4 = lane >> 4, c16 = lane & 15;
  volatile LAS unsigned* misc = (volatile LAS unsigned*)(smem + L_MISC);
  const __amdgpu_buffer_rsrc_t rws = mkrs(ws, WS_BYTES);
  const __amdgpu_buffer_rsrc_t rk = mkrs(Kc + (size_t)reg * (4096 * DH), 4096 * DH * 2);
  const __amdgpu_buffer_rsrc_t rx = mkrs(x, NB * DM * 2);
  const int voff = tid * 16;
  const int hw = wave - 4;
  const int uE = side * 8192, uO = uE ^ 8192;
#define ROFF(i, pE, pO) (((i) >> 1) * 16384 + ((i) & 1) * 4096 + ((((i) >> 1) & 1) ? (pO) : (pE)))
  v4u kb[PRE];
  const unsigned tstart = now32() + PACE2_OFF;
  if (wave < 4) {
#pragma unroll
    for (int i = 0; i < WIN; ++i) kb[i] = LD128(rk, voff, ROFF(i, uE, uO), AUX_NT);
  } else {
    const __amdgpu_buffer_rsrc_t rq = mkrs(wq + h * DH + (sp * 4 + b) * 16, (DM - 1) * (NH * DH * 2) + 32);
    kb[0] = LD128(rx, lane < 56 ? lane * 16 : (int)OOB, hw * (DM * 2), 0);
#pragma unroll
    for (int i = 0; i < 4; ++i) kb[1 + i] = LD128(rq, (lane >> 1) * (NH * DH * 2) + (lane & 1) * 16, (hw + 4 * i) * 32 * (NH * DH * 2), AUX_W);
    kb[5] = LD128(rws, 0, WS_EPOCH, 0);
#pragma unroll
    for (int i = 6; i < WIN; ++i) asm volatile("" : "=v"(kb[i]));
    if (wave == 4 && lane >= 8 && lane < 32) misc[lane] = 0u;
  }
  BARRIER();
  asm volatile("" : "+s"(wave));

  const int unit = blk & 63;
  const int oct = (b >> 1) * 16 + h, pj = (b & 1) * 4 + sp;
  if (wave < 4) {
    const __amdgpu_buffer_rsrc_t rv = mkrs(Vc + (size_t)reg * (4096 * DH), 4096 * DH * 2);
#pragma unroll
    for (int i = WIN; i < PRE; ++i) { USE(kb[i - WIN]); kb[i] = LD128(rk, voff, ROFF(i, uE, uO), AUX_NT); PIN(); }
    STAMP(1);
    BARRIER();
    STAMP(2);
    const v4u qv = *(const v4u*)(smem + L_QB + c32 * 16);
    const unsigned tag = __builtin_amdgcn_readfirstlane(misc[M_TAG]);
    float* scw = (float*)(smem + L_SC) + (wave * 2 + half) * 64 + (((c32 >> 4) & 1) | ((c32 >> 2) & 2) | (c32 & 4) | ((c32 << 2) & 8));
    const float* pvr = (const float*)(smem + L_PV) + (wave * 2 + half) * 64;
    const bool b3 = c32 & 8, b2 = c32 & 4, b1 = c32 & 2;
    float t0_, t1_, t2_, t3_;
#define COMB(CTRL, BIT, lo, hi) comb<CTRL>(BIT, lo, hi)
#define KSTEP(v, i, grp) do { \
      float d_ = dot2((v)[0], qv[0], 0.f); d_ = dot2((v)[1], qv[1], d_); d_ = dot2((v)[2], qv[2], d_); d_ = dot2((v)[3], qv[3], d_); \
      if (((i) & 1) == 0) t0_ = d_; else { d_ = swap16_add(t0_, d_); \
        if (((i) & 2) == 0) t1_ = d_; else { d_ = COMB(ROR8, b3, t1_, d_); \
          if (((i) & 4) == 0) t2_ = d_; else { d_ = COMB(XOR7, b2, t2_, d_); \
            if (((i) & 8) == 0) t3_ = d_; else { d_ = COMB(XOR2, b1, t3_, d_); d_ += dpp<XOR1>(d_); \
              scw[((grp) >> 2) * 512 + ((grp) & 3) * 16] = rbf(d_); } } } } \
      asm volatile("" : "+v"(t0_), "+v"(t1_), "+v"(t2_), "+v"(t3_)); } while (0)
#define PACE2_AT(row) do { \
      unsigned t_ = now32(); const unsigned due_ = tstart + (((unsigned)(row) * PACE2) >> 9); \
      SPIN(64) { if ((int)(t_ - due_) >= 0) break; __builtin_amdgcn_s_sleep(1); t_ = now32(); } } while (0)
    asm volatile("" : "=v"(t0_), "=v"(t1_), "=v"(t2_), "=v"(t3_));
    static_assert(PRE / 16 < GS && PRE <= 48, "placement switch");
#pragma unroll
    for (int i = 0; i < PRE; ++i) {
      REDEF(kb[i]);
      KSTEP(kb[i], i & 15, i >> 4);
      if (i < WIN) { PIN(); kb[i] = LD128(rk, voff, ROFF(PRE + i, uE, uO), AUX_NT); PIN(); }
    }
    TS(0);
    int aE = uE, aO = uO, sw = 0;
#pragma unroll 1
    for (int gi = PRE / 16; gi < 16; ++gi) {
      if (gi == GS - 1) {
        LDS_WAIT_GE(M_DEC, 1, 1);
        const unsigned dec = __builtin_amdgcn_readfirstlane(misc[M_DEC]);
        aE = (dec & 2u) << 12; aO = (dec & 4u) << 11; sw = (dec & 8u) << 10;
      }
      const int cE = gi * 131072 + (gi < GS ? uE : aE), cO = gi * 131072 + (gi < GS ? uO : aO);
      const __amdgpu_buffer_rsrc_t rn = gi < 15 ? rk : rv;
      const int nE = gi < 15 ? (gi + 1) * 131072 + (gi + 1 < GS ? uE : aE) : (uE ^ sw), nO = gi < 15 ? (gi + 1) * 131072 + (gi + 1 < GS ? uO : aO) : (uO ^ sw);
#pragma unroll
      for (int i = 0; i < 16; ++i) {
        if ((i & (PACE2_STEP - 1)) == 0) PACE2_AT(gi * 16 + i);
        USE(kb[i]);
        if (i + WIN < 16) kb[i + WIN] = LD128(rk, voff, ROFF(i + WIN, cE, cO), AUX_NT);
        else kb[i + WIN - 16] = LD128(rn, voff, ROFF(i + WIN - 16, nE, nO), AUX_NT);
        REDEF(kb[i]);
        KSTEP(kb[i], i, gi);
      }
      if ((gi & 3) == 3) { LGKM0(); if (lane == 0) LDS_INC(M_CNT_S); }
      if (gi & 1) TS(gi >> 1);
    }
    STAMP(3);
    float acc[8];
#pragma unroll
    for (int k = 0; k < 8; ++k) acc[k] = 0.f;
    LDS_WAIT_GE(M_P1, 1, 1);
#define VBODY(gi, LAST) do { \
      f32x4 p4[4]; \
      _Pragma("unroll") for (int i = 0; i < 4; ++i) p4[i] = *(const f32x4*)(pvr + ((gi) >> 2) * 512 + ((gi) & 3) * 16 + i * 4); \
      const int cE = (gi) * 131072 + (((gi) < GS ? uE : aE) ^ sw), cO = (gi) * 131072 + (((gi) < GS ? uO : aO) ^ sw); \
      const int nE = ((gi) + 1) * 131072 + (((gi) + 1 < GS ? uE : aE) ^ sw), nO = ((gi) + 1) * 131072 + (((gi) + 1 < GS ? uO : aO) ^ sw); \
      TS(8 + (gi)); \
      _Pragma("unroll") for (int i = 0; i < 16; ++i) { \
        if ((i & (PACE2_STEP - 1)) == 0 && (!(LAST) || i + WIN < 16)) PACE2_AT(256 + (gi) * 16 + i); \
        if ((LAST) && i == LATE_I && tid == 0) misc[M_LATE] = 1u; \
        USE(kb[i]); \
        if (i + WIN < 16) kb[i + WIN] = LD128(rv, voff, ROFF(i + WIN, cE, cO), AUX_NT); \
        else if (!(LAST)) kb[i + WIN - 16] = LD128(rv, voff, ROFF(i + WIN - 16, nE, nO), AUX_NT); \
        REDEF(kb[i]); \
        const float p = p4[i >> 2][i & 3]; \
        _Pragma("unroll") for (int k = 0; k < 4; ++k) { acc[2 * k] = fmaf(p, blo(kb[i][k]), acc[2 * k]); acc[2 * k + 1] = fmaf(p, bhi(kb[i][k]), acc[2 * k + 1]); } \
        asm volatile("" : "+v"(acc[0]), "+v"(acc[1]), "+v"(acc[2]), "+v"(acc[3]), "+v"(acc[4]), "+v"(acc[5]), "+v"(acc[6]), "+v"(acc[7])); \
      } } while (0)
#pragma unroll 1
    for (int gi = 0; gi < 15; ++gi) {
      if (gi == 12) {
        LDS_WAIT_GE(M_P2, 1, 1);
        const float alpha = u2f(misc[M_ALPHA]);
#pragma unroll
        for (int k = 0; k < 8; ++k) acc[k] *= alpha;
      }
      VBODY(gi, false);
    }
    VBODY(15, true);
    STAMP(4);
    {
      f32x4 o;
#pragma unroll
      for (int k = 0; k < 4; ++k) o[k] = swap32_add(acc[k], acc[k + 4]);
      f32x4* oa = (f32x4*)(smem + L_OA) + lane;
      oa[wave * 64] = o;
      LGKM0();
      unsigned old = 0;
      if (lane == 0) old = LDS_INC(M_CNT_O);
      if (__builtin_amdgcn_readfirstlane(old) == 3u) {
        PIN();
        o = (oa[0] + oa[64]) + (oa[128] + oa[192]);
        ST128(enc4(o, tag), rws, (c32 >> 2) * 1024 + (c32 & 3) * 32 + half * 16, WS_OX + oct * 8192 + pj * 128, AUX_SC1);
      }
    }
    STAMP(5);
    const int ru = unit - RU0;
    if (ru < 0 || ru >= NUNIT) RET();
    const v2u xr = LD64(rx, (lane & 7) * 8, b * (DM * 2) + ru * 64, 0);
    const int po = (lane >> 3) * (DM * 4) + (lane & 7) * 16;
    float* rs = (float*)(smem + L_RS);
    LDS_WAIT_GE(M_PDONE, 1, 1);
#pragma unroll 1
    for (int ph = 0; ph < 2; ++ph) {
      const unsigned ib = (ph == 0 ? WS_Y + (unsigned)(b * 128 + wave * 32) * (DM * 4u) : WS_O + (unsigned)(b * NM + wave * 28) * (DM * 4u)) + ru * 128;
      v4u p0 = {0u, 0u, 0u, 0u}, p1 = p0, p2 = p0, p3 = p0;
      bool k0 = false, k1 = false, k2 = false, k3 = ph == 1 && lane >= 32;
      SPIN(SPIN_CAP) {
        PIN();
        const v4u n0 = LD128(rws, k0 ? (int)OOB : po, ib, AUX_SC1), n1 = LD128(rws, k1 ? (int)OOB : po, ib + 8 * DM * 4, AUX_SC1);
        const v4u n2 = LD128(rws, k2 ? (int)OOB : po, ib + 16 * DM * 4, AUX_SC1), n3 = LD128(rws, k3 ? (int)OOB : po, ib + 24 * DM * 4, AUX_SC1);
        p0 = k0 ? p0 : n0; p1 = k1 ? p1 : n1; p2 = k2 ? p2 : n2; p3 = k3 ? p3 : n3;
        k0 = valid4(p0, tag); k1 = valid4(p1, tag); k2 = valid4(p2, tag); k3 |= valid4(p3, tag);
        if (__all(k0 & k1 & k2 & k3)) break;
      }
      if (ph == 0 || (wave & 1)) STAMP(6 + ph);
      const f32x4 v = (dec4(p0) + dec4(p1)) + (dec4(p2) + dec4(p3));
      float c = swap16_add(swap32_add(v[0], v[2]), swap32_add(v[1], v[3]));
      c += dpp<ROR8>(c);
      rs[wave * 32 + (lane & 7) * 4 + 2 * half + (g4 & 1)] = c;
      LGKM0();
      unsigned old = 0;
      if (lane == 0) old = LDS_INC(M_CNT_R);
      if (__builtin_amdgcn_readfirstlane(old) == 3u + 4u * ph) {
        PIN();
        const f32x4* r4 = (const f32x4*)rs + (lane & 7);
        const f32x4 sum = (r4[0] + r4[8]) + (r4[16] + r4[24]);
        f32x4 res;
        if (ph == 0) {
          res[0] = rbf(blo(xr[0]) + rbf(sum[0])); res[1] = rbf(bhi(xr[0]) + rbf(sum[1])); res[2] = rbf(blo(xr[1]) + rbf(sum[2])); res[3] = rbf(bhi(xr[1]) + rbf(sum[3]));
          ST128(enc4(res, tag), rws, (lane >> 3) * (NB * DM * 4) + (lane & 7) * 16, WS_HB + b * (DM * 4) + ru * 128, AUX_SC1);
          *((f32x4*)rs + 32 + (lane & 7)) = res;
          LGKM0();
          if (lane == 0) misc[M_RDONE] = 1u;
        } else {
          const f32x4 hq = r4[32];
          const v2u ov = {pk2(hq[0] + rbf(sum[0]), hq[1] + rbf(sum[1])), pk2(hq[2] + rbf(sum[2]), hq[3] + rbf(sum[3]))};
          ST64(ov, mkrs(out, NB * DM * 2), lane < 8 ? lane * 8 : (int)OOB, b * (DM * 2) + ru * 64, 0);
          if (blk == RU0) {
            const v2u ne = {misc[M_EP], misc[M_EP + 1]};
            ST64(ne, rws, lane == 0 ? 0 : (int)OOB, WS_EPOCH, AUX_SC1);
          }
        }
      }
      if (ph == 0) LDS_WAIT_GE(M_RDONE, 1, 1);
    }
    STAMP(1);
    RET();
  }
  const bool is_m = unit < 28;
  const int mi = unit * 4 + b;
  const int tq = c16 >> 2, tp = c16 & 3;
  const LAS unsigned char* s1 = (const LAS unsigned char*)(smem + L_W1) + (g4 * 8 + tq) * 32 + tp * 8;
  const unsigned char* hp = smem + L_HN + (c16 & 3) * HN_STRIDE + g4 * 16;
  {
    const unsigned char* pp = (const unsigned char*)(hw < 2 ? Kc : Vc) + ((size_t)reg << 21) + (3u << 19) + side * (1u << 18) + (hw & 1) * 8192;
    u64 ta, tb; unsigned dv;
    asm volatile("s_memtime %0\n\ts_waitcnt lgkmcnt(0)\n\t"
                 "s_load_dword %2, %3, 0x0\n\ts_waitcnt lgkmcnt(0)\n\t"
                 "s_memtime %1\n\ts_waitcnt lgkmcnt(0)" : "=&s"(ta), "=&s"(tb), "=&s"(dv) : "s"(pp) : "memory");
    if (lane == 0) misc[M_PL + hw] = (unsigned)(tb - ta);
  }
  {
    float v[8];
#pragma unroll
    for (int k = 0; k < 4; ++k) { v[2 * k] = blo(kb[0][k]); v[2 * k + 1] = bhi(kb[0][k]); }
    ln8(v, lane);
    const v4u xn = {pk2(v[0], v[1]), pk2(v[2], v[3]), pk2(v[4], v[5]), pk2(v[6], v[7])};
    if (lane < 56) *(v4u*)(smem + L_HN + hw * HN_STRIDE + lane * 16) = xn;
#pragma unroll
    for (int i = 0; i < 4; ++i) if (hw + 4 * i < 14) *(v4u*)(smem + L_W1 + (hw + 4 * i) * 1024 + lane * 16) = kb[1 + i];
    LGKM0();
    if (lane == 0) LDS_INC(M_CNT_Q);
  }
  const unsigned e0 = __builtin_amdgcn_readfirstlane(kb[5][0]), e1 = __builtin_amdgcn_readfirstlane(kb[5][1]);
  unsigned qtag = e0 + 0x9E3779B9u;
  qtag ^= qtag >> 16; qtag *= 0x85EBCA6Bu; qtag ^= qtag >> 13; qtag *= 0xC2B2AE35u; qtag ^= qtag >> 16;
  if (hw == 0) {
    LDS_WAIT_GE(M_CNT_Q, 4, 0);
    bf16x8 a1[14], bfr[14];
#pragma unroll
    for (int s = 0; s < 14; ++s) {
      a1[s] = __builtin_shufflevector(trd(s1 + s * 1024), trd(s1 + s * 1024 + 128), 0, 1, 2, 3, 4, 5, 6, 7);
      bfr[s] = *(const bf16x8*)(hp + s * 64);
    }
    const unsigned iod = ((unsigned)__builtin_amdgcn_s_getreg(20 | (3 << 11)) >> 2) & 1u;
    const unsigned nrm = iod ^ (unsigned)(reg & 1);
    const unsigned info = iod | ((((int)(misc[M_PL] - misc[M_PL + 1]) < 0 ? 0u : 1u) ^ nrm) << 1) | ((((int)(misc[M_PL + 2] - misc[M_PL + 3]) < 0 ? 0u : 1u) ^ nrm) << 2);
    __builtin_amdgcn_sched_barrier(0);
    f32x4 da = {0.f, 0.f, 0.f, 0.f}, db = {0.f, 0.f, 0.f, 0.f};
#pragma unroll
    for (int s = 0; s < 14; s += 2) {
      da = __builtin_amdgcn_mfma_f32_16x16x32_bf16(a1[s], bfr[s], da, 0, 0, 0);
      db = __builtin_amdgcn_mfma_f32_16x16x32_bf16(a1[s + 1], bfr[s + 1], db, 0, 0, 0);
    }
    da += db;
    const v4u qg = {pk2(da[0], da[1]), (qtag & ~7u) | info, pk2(da[2], da[3]), qtag};
    ST128(qg, rws, c16 < 4 ? c16 * 64 + g4 * 16 : (int)OOB, WS_Q + h * 4096 + (sp * 4 + b) * 256, AUX_SC1);
    if (lane == 0) LDS_INC(M_CNT_Q);
    STAMP(1);
  } else if (hw == 3) {
    if (lane == 0) { misc[M_TAG] = qtag; misc[M_EP] = e0 + 1u; misc[M_EP + 1] = e1; }
  } else if (hw == 1) {
    v4u qg;
    LDS_WAIT_GE(M_CNT_Q, 5, 1);
    SPIN(SPIN_CAP) {
      PIN();
      qg = LD128(rws, (lane >> 2) * 256 + (lane & 3) * 16, WS_Q + h * 4096 + b * 64, AUX_SC1);
      if (__all((qg[1] ^ qtag) < 8u && qg[3] == qtag)) break;
    }
    *(v2u*)(smem + L_QB + lane * 8) = v2u{qg[0], qg[2]};
    STAMP(1);
  }
  BARRIER();
  if (hw == 2) {
    unsigned dec = 1u | (side << 1) | ((side ^ 1) << 2);
    unsigned inf[4];
    SPIN(SPIN_CAP) {
      PIN();
      bool ok = true;
#pragma unroll
      for (int i = 0; i < 4; ++i) { inf[i] = __builtin_amdgcn_raw_buffer_load_b32(rws, (lane >> 2) * 4096 + (lane & 3) * 1024 + 4, WS_Q + i * 256, AUX_SC1); ok &= (inf[i] ^ qtag) < 8u; }
      if (__all(ok)) break;
      __builtin_amdgcn_s_sleep(8);
    }
    int nk = 0, nv = 0, lo = 64, hi = 0, lov = 64, hiv = 0;
#pragma unroll
    for (int i = 0; i < 4; ++i) {
      const int a = __builtin_popcountll(__ballot(inf[i] & 2u)), c = __builtin_popcountll(__ballot(inf[i] & 4u));
      nk += a; nv += c; lo = min(lo, a); hi = max(hi, a); lov = min(lov, c); hiv = max(hiv, c);
    }
    const unsigned own = b == 0 ? inf[0] : b == 1 ? inf[1] : b == 2 ? inf[2] : inf[3];
    const unsigned i0 = __builtin_amdgcn_readlane(own, h * 4 + sp) & 1u, i1 = __builtin_amdgcn_readlane(own, h * 4 + (sp ^ 1)) & 1u;
    const bool aligned = (((u64)Kc | (u64)Vc) & 0x1FFFFFull) == 0;
    const bool okk = (nk >= VOTE_THR && lo >= POOL_THR) || (nk <= 256 - VOTE_THR && hi <= 64 - POOL_THR);
    const bool okv = (nv >= VOTE_THR && lov >= POOL_THR) || (nv <= 256 - VOTE_THR && hiv <= 64 - POOL_THR);
    if (aligned && i0 != i1 && okk && okv) {
      const unsigned ck = nk > 128, cv = nv > 128, pref = ck ^ (unsigned)(reg & 1) ^ i0;
      dec = 1u | (pref << 1) | (pref << 2) | ((ck ^ cv) << 3);
    }
    nk = min(nk, 255); nv = min(nv, 255);
    if (lane == 0) misc[M_DEC] = dec;
    STAMP(1);
  }
  bf16x8 a0[8];
  {
    const __amdgpu_buffer_rsrc_t r0 = mkrs(wo + (size_t)(h * DH + pj * 32) * DM, 32 * DM * 2);
    v4u t0[7];
#pragma unroll
    for (int i = 0; i < 7; ++i) t0[i] = LD128(r0, lane * 16, (hw + 4 * i) * 1024, AUX_W);
    const unsigned d1 = __builtin_amdgcn_raw_buffer_load_b16(mkrs(hw == 0 ? w1 : hw == 1 ? w2 : hw == 2 ? w1 + DM * FF - 64 : w2 + DM * FF - 64, 64), 0, 0, 0);
#pragma unroll
    for (int i = 0; i < 7; ++i) *(v4u*)(smem + L_WO + (hw + 4 * i) * 1024 + lane * 16) = t0[i];
    USE(d1);
    LGKM0();
    if (lane == 0) LDS_INC(M_CNT_W);
    LDS_WAIT_GE(M_CNT_W, 4, 2);
    const LAS unsigned char* s0 = (const LAS unsigned char*)(smem + L_WO) + (g4 * 8 + tq) * (DM * 2) + (hw * 64 + tp * 4) * 2;
#pragma unroll
    for (int t = 0; t < 8; ++t) a0[t] = __builtin_shufflevector(trd(s0 + (t & 3) * 32 + (t >> 2) * 512), trd(s0 + (t & 3) * 32 + (t >> 2) * 512 + 4 * DM * 2), 0, 1, 2, 3, 4, 5, 6, 7);
#pragma unroll
    for (int t = 0; t < 8; ++t) asm volatile("" : "+v"(a0[t]));
  }
  if (hw < 2) {
    LDS_WAIT_GE(M_DEC, 1, 4);
    const bool swp = __builtin_amdgcn_readfirstlane(misc[M_DEC]) & 8u;
    const f32x4* scl = (const f32x4*)(smem + L_SC) + lane * 2;
    if (hw == 1) {
      if (swp) {
#pragma unroll 1
        for (int sg = 0; sg < 4; ++sg) {
          LDS_WAIT_GE(M_CNT_S, 4 * (sg + 1), 8);
          const f32x4 a = scl[sg * 128], c = scl[sg * 128 + 1];
          const v4u pk = {__builtin_amdgcn_perm(f2u(a[1]), f2u(a[0]), 0x07060302u), __builtin_amdgcn_perm(f2u(a[3]), f2u(a[2]), 0x07060302u),
                          __builtin_amdgcn_perm(f2u(c[1]), f2u(c[0]), 0x07060302u), __builtin_amdgcn_perm(f2u(c[3]), f2u(c[2]), 0x07060302u)};
          ST128(pk, rws, lane * 16, WS_SX + blk * 8192 + sg * 1024, SXMODE == 2 ? 0 : AUX_SC1);
          if (sg >= 2) {
            DRAIN();
            ST32(qtag, rws, lane == 0 ? 0 : (int)OOB, WS_SX + blk * 8192 + 4096 + (sg - 2) * 128, AUX_SC1);
          }
        }
      }
    } else {
      f32x4* pvl = (f32x4*)(smem + L_PV) + lane * 2;
      f32x4 sv[6];
#define UNPK(dst0, dst1, r) do { dst0 = f32x4{blo(r[0]), bhi(r[0]), blo(r[1]), bhi(r[1])}; dst1 = f32x4{blo(r[2]), bhi(r[2]), blo(r[3]), bhi(r[3])}; } while (0)
      LDS_WAIT_GE(M_CNT_S, 12, 8);
      if (swp) {
        SPIN(SPIN_CAP) {
          __builtin_amdgcn_s_sleep(32);
          PIN();
          if (__builtin_amdgcn_readfirstlane(__builtin_amdgcn_raw_buffer_load_b32(rws, 0, WS_SX + (blk ^ 4) * 8192 + 4096, AUX_SC1)) == qtag) break;
        }
        PIN();
#pragma unroll
        for (int i = 0; i < 3; ++i) { const v4u r = LD128(rws, lane * 16, WS_SX + (blk ^ 4) * 8192 + i * 1024, AUX_SC1); UNPK(sv[2 * i], sv[2 * i + 1], r); }
      } else {
#pragma unroll
        for (int i = 0; i < 6; ++i) sv[i] = scl[(i >> 1) * 128 + (i & 1)];
      }
      float m1 = -INFINITY;
#pragma unroll
      for (int i = 0; i < 6; ++i) m1 = fmaxf(m1, fmaxf(fmaxf(sv[i][0], sv[i][1]), fmaxf(sv[i][2], sv[i][3])));
      m1 = wave_max(m1);
      float l = 0.f;
#pragma unroll
      for (int i = 0; i < 6; ++i) {
#pragma unroll
        for (int e = 0; e < 4; ++e) { sv[i][e] = __builtin_amdgcn_exp2f((sv[i][e] - m1) * SCL2E); l += sv[i][e]; }
        pvl[(i >> 1) * 128 + (i & 1)] = sv[i];
      }
      LGKM0();
      if (lane == 0) misc[M_P1] = 1u;
      LDS_WAIT_GE(M_CNT_S, 16, 8);
      if (swp) {
        SPIN(SPIN_CAP) {
          __builtin_amdgcn_s_sleep(32);
          PIN();
          if (__builtin_amdgcn_readfirstlane(__builtin_amdgcn_raw_buffer_load_b32(rws, 0, WS_SX + (blk ^ 4) * 8192 + 4096 + 128, AUX_SC1)) == qtag) break;
        }
        PIN();
        const v4u r = LD128(rws, lane * 16, WS_SX + (blk ^ 4) * 8192 + 3072, AUX_SC1); UNPK(sv[0], sv[1], r);
      } else {
        sv[0] = scl[384]; sv[1] = scl[385];
      }
      float m = m1;
#pragma unroll
      for (int i = 0; i < 2; ++i) m = fmaxf(m, fmaxf(fmaxf(sv[i][0], sv[i][1]), fmaxf(sv[i][2], sv[i][3])));
      m = wave_max(m);
      const float alpha = __builtin_amdgcn_exp2f((m1 - m) * SCL2E);
      l *= alpha;
#pragma unroll
      for (int i = 0; i < 2; ++i) {
#pragma unroll
        for (int e = 0; e < 4; ++e) { sv[i][e] = __builtin_amdgcn_exp2f((sv[i][e] - m) * SCL2E); l += sv[i][e]; }
        pvl[384 + i] = sv[i];
      }
      l = wave_sum(l);
      if (lane == 0) { misc[M_MS] = f2u(m); misc[M_MS + 1] = f2u(l); misc[M_ALPHA] = f2u(alpha); }
      LGKM0();
      if (lane == 0) misc[M_P2] = 1u;
      const unsigned dummy = __builtin_amdgcn_raw_buffer_load_b16(mkrs(out, NB * DM * 2), 0, 0, 0);
      USE(dummy);
    }
  }
  LDS_WAIT_GE(M_LATE, 1, 4);
  if (hw == 0) {
    const f32x4 mlv = {u2f(misc[M_MS]), u2f(misc[M_MS + 1]), 0.f, 0.f};
    ST128(enc4(mlv, qtag), rws, lane < 8 ? lane * 1024 : (int)OOB, WS_ML + oct * 8192 + pj * 128, AUX_SC1);
  }
  v4u t1[4], t2[4];
  {
    const __amdgpu_buffer_rsrc_t r1 = mkrs(w1 + mi * 16, is_m ? (DM - 1) * (FF * 2) + 32 : 0);
    const __amdgpu_buffer_rsrc_t r2 = mkrs(w2 + (size_t)mi * 16 * DM, is_m ? 16 * DM * 2 : 0);
#pragma unroll
    for (int i = 0; i < 4; ++i) {
      t1[i] = LD128(r1, (lane >> 1) * (FF * 2) + (lane & 1) * 16, (hw + 4 * i) * 32 * (FF * 2), AUX_W);
      t2[i] = LD128(r2, lane * 16, (hw + 4 * i) * 1024, AUX_W);
    }
    PIN();
  }
  STAMP(3);
  {
    v4u pv;
    SPIN(SPIN_CAP) {
      PIN();
      pv = LD128(rws, (c16 & 7) * 128, WS_ML + oct * 8192 + pj * 1024, AUX_SC1);
      if (__all(valid4(pv, qtag))) break;
      __builtin_amdgcn_s_sleep(1);
    }
    float wn;
    {
      const float ms = u2f(pv[0] & 0xFFFFFF00u), ls = u2f((pv[1] & 0xFFFFFF00u) | 0x80u);
      float M = fmaxf(ms, dpp<XOR1>(ms)); M = fmaxf(M, dpp<XOR2>(M));
      const float e = __builtin_amdgcn_exp2f((ms - M) * SCL2E);
      float L = ls * e; L += dpp<XOR1>(L); L += dpp<XOR2>(L);
      wn = e * __builtin_amdgcn_rcpf(L);
    }
    const int po = ((c16 >> 2) & 1) * 512 + (c16 & 3) * 128 + g4 * 32 + (c16 >> 3) * 16;
    SPIN(SPIN_CAP) {
      PIN();
      pv = LD128(rws, po, WS_OX + oct * 8192 + pj * 1024, AUX_SC1);
      if (__all(valid4(pv, qtag))) break;
    }
    STAMP(5);
    f32x4 o = dec4(pv);
#pragma unroll
    for (int k = 0; k < 4; ++k) { o[k] *= wn; o[k] += dpp<XOR1>(o[k]); o[k] += dpp<XOR2>(o[k]); }
    v4u bo;
    bo[0] = pk2(o[0], o[1]); bo[1] = pk2(o[2], o[3]);
    bo[2] = f2u(dpp<ROR8>(u2f(bo[0]))); bo[3] = f2u(dpp<ROR8>(u2f(bo[1])));
    f32x4 y[8];
#pragma unroll
    for (int t = 0; t < 8; ++t) y[t] = __builtin_amdgcn_mfma_f32_16x16x32_bf16(a0[t], __builtin_bit_cast(bf16x8, bo), f32x4{0.f, 0.f, 0.f, 0.f}, 0, 0, 0);
    __builtin_amdgcn_sched_barrier(0);
    const int vo = c16 < 8 ? (c16 >> 2) * (128 * DM * 4) + (c16 & 3) * 64 + g4 * 16 : (int)OOB;
    const bool s1b = c16 & 1, s2b = c16 & 2;
    const unsigned yb = WS_Y + (unsigned)((b & 2) * 128 + h * 8 + pj) * (DM * 4u) + hw * 256;
    ST128(enc4(sel4(s1b, s2b, y[0], y[1], y[2], y[3]), qtag), rws, vo, yb, AUX_SC1);
    ST128(enc4(sel4(s1b, s2b, y[4], y[5], y[6], y[7]), qtag), rws, hw < 3 ? vo : (int)OOB, yb + 1024, AUX_SC1);
    if (lane == 0) misc[M_PDONE] = 1u;
  }
  STAMP(6);
  if (!is_m) RET();
  bf16x8 a1[14];
  s16x4 a2[8];
  {
#pragma unroll
    for (int i = 0; i < 4; ++i) if (hw + 4 * i < 14) {
      *(v4u*)(smem + L_W1 + (hw + 4 * i) * 1024 + lane * 16) = t1[i];
      *(v4u*)(smem + L_W2 + (hw + 4 * i) * 1024 + lane * 16) = t2[i];
    }
    LGKM0();
    if (lane == 0) LDS_INC(M_CNT_W);
    LDS_WAIT_GE(M_CNT_W, 8, 0);
    const LAS unsigned char* s2 = (const LAS unsigned char*)(smem + L_W2) + (g4 * 4 + tq) * (DM * 2) + (hw * 64 + tp * 4) * 2;
#pragma unroll
    for (int t = 0; t < 8; ++t) a2[t] = __builtin_bit_cast(s16x4, trd(s2 + (t & 3) * 32 + (t >> 2) * 512));
    asm volatile("" : "+v"(a2[7]));
#pragma unroll
    for (int s = 0; s < 14; ++s) a1[s] = __builtin_shufflevector(trd(s1 + s * 1024), trd(s1 + s * 1024 + 128), 0, 1, 2, 3, 4, 5, 6, 7);
#pragma unroll
    for (int t = 0; t < 7; ++t) asm volatile("" : "+v"(a2[t]), "+v"(a1[2 * t]), "+v"(a1[2 * t + 1]));
  }
  v4u h0, h1;
  {
    const unsigned hb = WS_HB + (unsigned)(blk & 7) * (NB * DM * 4) + hw * (DM * 4);
    const int vo = lane < 56 ? lane * 32 : (int)OOB;
    SPIN(SPIN_CAP) {
      PIN();
      h0 = LD128(rws, vo, hb, AUX_SC1); h1 = LD128(rws, vo, hb + 16, AUX_SC1);
      const bool ok = (valid4(h0, qtag) & valid4(h1, qtag)) | (lane >= 56);
      if (__all(ok)) break;
    }
  }
  STAMP(7);
  {
    float v[8];
#pragma unroll
    for (int k = 0; k < 4; ++k) { v[k] = bhi(h0[k]); v[k + 4] = bhi(h1[k]); }
    ln8(v, lane);
    const v4u hn = {pk2(v[0], v[1]), pk2(v[2], v[3]), pk2(v[4], v[5]), pk2(v[6], v[7])};
    if (lane < 56) *(v4u*)(smem + L_HN + hw * HN_STRIDE + lane * 16) = hn;
    LGKM0();
    if (lane == 0) LDS_INC(M_CNT_HN);
    LDS_WAIT_GE(M_CNT_HN, 4, 0);
  }
  f32x4 d1a = {0.f, 0.f, 0.f, 0.f}, d1b = {0.f, 0.f, 0.f, 0.f};
  {
    bf16x8 bfr[14];
#pragma unroll
    for (int s = 0; s < 14; ++s) bfr[s] = *(const bf16x8*)(hp + s * 64);
    __builtin_amdgcn_sched_barrier(0);
#pragma unroll
    for (int s = 0; s < 14; s += 2) {
      d1a = __builtin_amdgcn_mfma_f32_16x16x32_bf16(a1[s], bfr[s], d1a, 0, 0, 0);
      d1b = __builtin_amdgcn_mfma_f32_16x16x32_bf16(a1[s + 1], bfr[s + 1], d1b, 0, 0, 0);
    }
  }
  float sv[4];
#pragma unroll
  for (int r = 0; r < 4; ++r) { const float t = rbf(d1a[r] + d1b[r]); sv[r] = t * __builtin_amdgcn_rcpf(1.0f + __builtin_amdgcn_exp2f(-t * LOG2E)); }
  const v2u hbp = {pk2(sv[0], sv[1]), pk2(sv[2], sv[3])};
  const s16x4 b2v = __builtin_bit_cast(s16x4, hbp);
  const int vo = (c16 & 3) * (NM * DM * 4) + (c16 >> 2) * 64 + g4 * 16;
  f32x4 o2[8];
#pragma unroll
  for (int t = 0; t < 8; ++t) o2[t] = __builtin_amdgcn_mfma_f32_16x16x16bf16_1k(a2[t], b2v, f32x4{0.f, 0.f, 0.f, 0.f}, 0, 0, 0);
  __builtin_amdgcn_sched_barrier(0);
  {
    const bool s1b = c16 & 4, s2b = c16 & 8;
    const unsigned ob = WS_O + (unsigned)mi * (DM * 4u) + hw * 256;
    ST128(enc4(sel4(s1b, s2b, o2[0], o2[1], o2[2], o2[3]), qtag), rws, vo, ob, AUX_SC1);
    ST128(enc4(sel4(s1b, s2b, o2[4], o2[5], o2[6], o2[7]), qtag), rws, hw < 3 ? vo : (int)OOB, ob + 1024, AUX_SC1);
  }
  STAMP(2);
  RET();
}

extern "C" void kernel_launch(void* const* d_in, const int* in_sizes, int n_in, void* d_out, int out_size, void* d_ws, size_t ws_size, hipStream_t stream) {
  fused_decode<<<NWG, NTHR, 0, stream>>>((const u16*)d_in[0], (const u16*)d_in[1], (const u16*)d_in[2], (const u16*)d_in[3], (const u16*)d_in[4],
                                         (const u16*)d_in[6], (const u16*)d_in[7], (u16*)d_out, (unsigned char*)d_ws);
}
```

```cpp
#include <hip/hip_runtime.h>
#include <stdint.h>

typedef unsigned v4u __attribute__((ext_vector_type(4)));
typedef unsigned v2u __attribute__((ext_vector_type(2)));
typedef float f32x4 __attribute__((ext_vector_type(4)));
typedef __bf16 bf16x2 __attribute__((ext_vector_type(2)));
typedef __bf16 bf16x4 __attribute__((ext_vector_type(4)));
typedef __bf16 bf16x8 __attribute__((ext_vector_type(8)));
typedef short s16x4 __attribute__((ext_vector_type(4)));
typedef unsigned short u16;
typedef unsigned long long u64;
#define LAS __attribute__((address_space(3)))

constexpr int NB = 4, DM = 448, NH = 16, SK = 8192, DH = 256, FF = 1792;
constexpr int NWG = 256, NTHR = 512;
constexpr int FIN = 16;
constexpr int WIN = 8;
constexpr int PRE = 48;
constexpr int NUNIT = 14;
constexpr int NM = 112;
constexpr int RU0 = 32;
constexpr int AUX_NT = 2, AUX_SC1 = 16;
constexpr unsigned PACE = 6925;
constexpr unsigned PACE_OFF = 120, PACE_RAMP = 75;
constexpr unsigned PER = 32, CH0 = 16, NCHK = 14, RDROWS = 20;
constexpr unsigned DEAD = 30, LOWL = 12, INCMAX = 64, BONUS = 0;
constexpr int SLEW = 8;
constexpr unsigned P_OWN = 60;
constexpr unsigned OOB = 0x7FFFFFF0u;
constexpr float LOG2E = 1.4426950408889634f;
constexpr float SCL2E = 0.0625f * LOG2E;

constexpr unsigned WS_EPOCH = 0, WS_ML = 8192, WS_HB = 16384, WS_Q = 1u << 17, WS_SX = 3u << 16, WS_OX = 3u << 17, WS_Y = 5u << 17,
                   WS_O = 9u << 17, WS_LAG = 31u << 16, WS_BYTES = 2u << 20;
static_assert(WS_SX + 256 * 512 <= WS_OX && WS_OX + 256 * 1024 <= WS_Y && WS_Y + 256 * DM * 4 <= WS_O && WS_O + NB * NM * DM * 4 <= WS_LAG && WS_LAG + 32 * 2048 <= WS_BYTES, "ws");
constexpr int L_WO = 0;
constexpr int L_W1 = L_WO + 57344;
constexpr int L_W2 = L_W1 + 14336;
constexpr int L_SC = L_W2 + 14336;
constexpr int L_PV = L_SC + 8192;
constexpr int L_PF = L_PV + 8192;
constexpr int L_QB = L_PF + 2048;
constexpr int L_OA = L_QB + 512;
constexpr int L_FN = L_OA + 4096;
constexpr int L_RS = L_FN + 1024;
constexpr int L_HN = L_RS + 1024;
constexpr int HN_STRIDE = 912;
constexpr int L_MISC = L_HN + 4 * HN_STRIDE;
constexpr int L_BYTES = L_MISC + 128;
static_assert(L_BYTES > 81920 && L_BYTES <= 163840 && L_MISC % 16 == 0 && L_HN % 16 == 0, "lds");
enum { M_ALPHA = 0, M_TAG = 1, M_EP = 2, M_CNT_S = 8, M_CNT_S2, M_P1, M_P2, M_QOK, M_CNT_HN, M_RDONE, M_CNT_Q, M_CNT_O, M_OPUB, M_CNT_W, M_PDONE, M_CNT_R, M_MID, M_PFOK, M_CNT_FN, M_SHIFT, M_PROG };

__device__ __forceinline__ float u2f(unsigned u) { return __builtin_bit_cast(float, u); }
__device__ __forceinline__ unsigned f2u(float f) { return __builtin_bit_cast(unsigned, f); }
__device__ __forceinline__ float rbf(float f) { return (float)(__bf16)f; }
__device__ __forceinline__ unsigned pk2(float lo, float hi) { bf16x2 v = {(__bf16)lo, (__bf16)hi}; return __builtin_bit_cast(unsigned, v); }
__device__ __forceinline__ float blo(unsigned u) { return u2f(u << 16); }
__device__ __forceinline__ float bhi(unsigned u) { return u2f(u & 0xffff0000u); }
__device__ __forceinline__ float dot2(unsigned a, unsigned b, float c) {
  return __builtin_amdgcn_fdot2_f32_bf16(__builtin_bit_cast(bf16x2, a), __builtin_bit_cast(bf16x2, b), c, false);
}
template <int CTRL> __device__ __forceinline__ float dpp(float x) {
  return __builtin_bit_cast(float, __builtin_amdgcn_mov_dpp(__builtin_bit_cast(int, x), CTRL, 0xf, 0xf, true));
}
constexpr int XOR1 = 0xB1, XOR2 = 0x4E, XOR7 = 0x141, ROR8 = 0x128, ROR4 = 0x124;
__device__ __forceinline__ float swap16_add(float a, float b) {
  auto s = __builtin_amdgcn_permlane16_swap(f2u(a), f2u(b), false, false);
  const unsigned s0 = s[0], s1 = s[1];
  return u2f(s0) + u2f(s1);
}
__device__ __forceinline__ float swap32_add(float a, float b) {
  auto s = __builtin_amdgcn_permlane32_swap(f2u(a), f2u(b), false, false);
  const unsigned s0 = s[0], s1 = s[1];
  return u2f(s0) + u2f(s1);
}
__device__ __forceinline__ float swap16_max(float a) {
  auto s = __builtin_amdgcn_permlane16_swap(f2u(a), f2u(a), false, false);
  const unsigned s0 = s[0], s1 = s[1];
  return fmaxf(u2f(s0), u2f(s1));
}
__device__ __forceinline__ float swap32_max(float a) {
  auto s = __builtin_amdgcn_permlane32_swap(f2u(a), f2u(a), false, false);
  const unsigned s0 = s[0], s1 = s[1];
  return fmaxf(u2f(s0), u2f(s1));
}
__device__ __forceinline__ float row_sum(float x) { x += dpp<ROR8>(x); x += dpp<ROR4>(x); x += dpp<XOR2>(x); x += dpp<XOR1>(x); return x; }
__device__ __forceinline__ float row_max(float x) { x = fmaxf(x, dpp<ROR8>(x)); x = fmaxf(x, dpp<ROR4>(x)); x = fmaxf(x, dpp<XOR2>(x)); x = fmaxf(x, dpp<XOR1>(x)); return x; }
__device__ __forceinline__ float wave_sum(float x) { x = row_sum(x); x = swap16_add(x, x); return swap32_add(x, x); }
__device__ __forceinline__ float wave_max(float x) { x = row_max(x); x = swap16_max(x); return swap32_max(x); }

__device__ __forceinline__ __amdgpu_buffer_rsrc_t mkrs(const void* p, unsigned bytes) { return __builtin_amdgcn_make_buffer_rsrc((void*)p, 0, bytes, 0x00020000); }
#define LD128(rs, vo, so, aux) __builtin_amdgcn_raw_buffer_load_b128(rs, vo, so, aux)
#define LD64(rs, vo, so, aux) __builtin_amdgcn_raw_buffer_load_b64(rs, vo, so, aux)
#define ST128(v, rs, vo, so, aux) do { const v4u sv_ = (v); __builtin_amdgcn_raw_buffer_store_b128(sv_, rs, vo, so, aux); asm volatile("s_nop 1" :: "v"(sv_) : "memory"); } while (0)
#define ST64(v, rs, vo, so, aux) __builtin_amdgcn_raw_buffer_store_b64(v, rs, vo, so, aux)
#define ST32(v, rs, vo, so, aux) __builtin_amdgcn_raw_buffer_store_b32(v, rs, vo, so, aux)
#define PIN() asm volatile("" ::: "memory")
#define USE(x) asm volatile("" :: "v"(x) : "memory")
#define REDEF(x) asm volatile("" : "+v"(x) :: "memory")
#define BARRIER() asm volatile("s_waitcnt lgkmcnt(0)\n\ts_barrier" ::: "memory")
constexpr int SPIN_CAP = 1 << 16;
constexpr int LSPIN_CAP = 1 << 20;

__device__ __forceinline__ void ln_tail(float (&v)[8], float piv, float s, float q) {
  float t = swap32_add(s, q);
  t = swap16_add(t, t);
  t = row_sum(t);
  const float ms = u2f(__builtin_amdgcn_readlane(f2u(t), 0)) * (1.0f / DM), mq = u2f(__builtin_amdgcn_readlane(f2u(t), 32)) * (1.0f / DM);
  const float var = fmaxf(fmaf(-ms, ms, mq), 0.f);
  const float mb = rbf(piv + ms);
  const float den = rbf(__builtin_amdgcn_sqrtf(rbf(rbf(var) + 1e-5f)));
  const float inv = __builtin_amdgcn_rcpf(den);
#pragma unroll
  for (int e = 0; e < 8; ++e) v[e] = rbf(v[e] - mb) * inv;
}
__device__ __forceinline__ void ln8(float (&v)[8], int lane) {
  const float piv = u2f(__builtin_amdgcn_readfirstlane(f2u(v[0])));
  const float pv2 = lane < 56 ? piv : 0.f;
  float d[8];
#pragma unroll
  for (int e = 0; e < 8; ++e) d[e] = v[e] - pv2;
  const float s = ((d[0] + d[1]) + (d[2] + d[3])) + ((d[4] + d[5]) + (d[6] + d[7]));
  const float q = (fmaf(d[0], d[0], d[1] * d[1]) + fmaf(d[2], d[2], d[3] * d[3])) + (fmaf(d[4], d[4], d[5] * d[5]) + fmaf(d[6], d[6], d[7] * d[7]));
  ln_tail(v, piv, s, q);
}
__device__ __forceinline__ void ln8b(float (&v)[8], int lane) {
  const float piv = u2f(__builtin_amdgcn_readfirstlane(f2u(v[0])));
  const float pv2 = lane < 48 ? piv : 0.f;
  float d[8];
#pragma unroll
  for (int e = 0; e < 4; ++e) { d[e] = v[e] - piv; d[e + 4] = v[e + 4] - pv2; }
  float s = ((d[0] + d[1]) + (d[2] + d[3])) + ((d[4] + d[5]) + (d[6] + d[7]));
  float q = (fmaf(d[0], d[0], d[1] * d[1]) + fmaf(d[2], d[2], d[3] * d[3])) + (fmaf(d[4], d[4], d[5] * d[5]) + fmaf(d[6], d[6], d[7] * d[7]));
  ln_tail(v, piv, s, q);
}
__device__ __forceinline__ v4u enc4(f32x4 v, unsigned tag) {
  v4u r;
  r[0] = __builtin_amdgcn_perm(f2u(v[0]), tag, 0x07060500u); r[1] = __builtin_amdgcn_perm(f2u(v[1]), tag, 0x07060501u);
  r[2] = __builtin_amdgcn_perm(f2u(v[2]), tag, 0x07060502u); r[3] = __builtin_amdgcn_perm(f2u(v[3]), tag, 0x07060503u);
  return r;
}
__device__ __forceinline__ bool valid4(v4u a, unsigned tag) {
  return (__builtin_amdgcn_perm(a[1], a[0], 0x0c0c0400u) | __builtin_amdgcn_perm(a[3], a[2], 0x04000c0cu)) == tag;
}
__device__ __forceinline__ f32x4 dec4(v4u a) { return __builtin_bit_cast(f32x4, (a & 0xFFFFFF00u) | 0x80u); }
__device__ __forceinline__ f32x4 dec4x(v4u a) { return __builtin_bit_cast(f32x4, a & 0xFFFFFF00u); }

#define SPIN(cap) _Pragma("unroll 1") for (int spin = 0; spin < (cap); ++spin)
#define LDS_INC(w) __hip_atomic_fetch_add((LAS unsigned*)(smem + L_MISC) + (w), 1u, __ATOMIC_RELAXED, __HIP_MEMORY_SCOPE_WORKGROUP)
#define LDS_WAIT_GE(w, n, slp) do { SPIN(LSPIN_CAP) { PIN(); if (misc[w] >= (unsigned)(n)) break; if (slp) __builtin_amdgcn_s_sleep(slp); } PIN(); } while (0)
template <int CTRL> __device__ __forceinline__ float comb(bool bit, float lo, float hi) {
  const float a = lo + dpp<CTRL>(lo), b = hi + dpp<CTRL>(hi);
  return bit ? b : a;
}
__device__ __forceinline__ f32x4 sel4(bool s1, bool s2, f32x4 a, f32x4 b, f32x4 c, f32x4 d) {
  f32x4 r;
#pragma unroll
  for (int k = 0; k < 4; ++k) { const float lo = s1 ? b[k] : a[k], hi = s1 ? d[k] : c[k]; r[k] = s2 ? hi : lo; }
  return r;
}
__device__ __forceinline__ bf16x4 trd(const LAS unsigned char* p) { return __builtin_amdgcn_ds_read_tr16_b64_v4bf16((LAS bf16x4*)p); }
__device__ __forceinline__ unsigned now32() { return (unsigned)__builtin_amdgcn_s_memrealtime(); }

__global__ void __launch_bounds__(NTHR, 2)
fused_decode(const u16* x, const u16* Kc, const u16* Vc, const u16* wq, const u16* wo, const u16* w1, const u16* w2, u16* out, unsigned char* ws) {
  __shared__ __attribute__((aligned(16))) unsigned char smem[L_BYTES];
  const int tid = threadIdx.x, lane = tid & 63;
  int wave = __builtin_amdgcn_readfirstlane(tid >> 6);
  const int blk = blockIdx.x;
  const int b = blk >> 6, h = ((blk >> 3) & 7) * 2 + ((blk >> 1) & 1), side = (blk >> 2) & 1, sp = (blk & 1) * 2 + side;
  const int reg = (blk >> 3) * 4 + (blk & 3), bh = reg >> 1;
  const int ds = (sp + (blk >> 3)) & 3;
  const int qs = (((blk & 7) * 8 + ((blk >> 3) & 7)) << 2) + b;
  const int half = lane >> 5, c32 = lane & 31, g4 = lane >> 4, c16 = lane & 15;
  volatile LAS unsigned* misc = (volatile LAS unsigned*)(smem + L_MISC);
  const __amdgpu_buffer_rsrc_t rws = mkrs(ws, WS_BYTES);
  const __amdgpu_buffer_rsrc_t rk = mkrs(Kc + (size_t)reg * (4096 * DH), 4096 * DH * 2);
  const __amdgpu_buffer_rsrc_t rx = mkrs(x, NB * DM * 2);
  const int voff = tid * 16;
  const int hw = wave - 4;
  const int uE = side * 8192, uO = uE ^ 8192;
#define ROFF(i, pE, pO) (((i) >> 1) * 16384 + ((i) & 1) * 4096 + ((((i) >> 1) & 1) ? (pO) : (pE)))
  v4u kb[PRE];
  unsigned cpf = 0;
  {
    u64 pc0; unsigned clen, clen2;
    asm volatile("s_getpc_b64 %0\n.Lfd_code_begin:\n\ts_mov_b32 %1, .Lfd_code_end-.Lfd_code_begin\n\ts_mov_b32 %2, .Lfd_code_end2-.Lfd_code_begin"
                 : "=s"(pc0), "=s"(clen), "=s"(clen2));
    const __amdgpu_buffer_rsrc_t rc = mkrs((const void*)pc0, (blk & 0x38) == 0x20 ? max(clen, clen2) : 0u);
    if (wave >= 4) { cpf = __builtin_amdgcn_raw_buffer_load_b32(rc, lane * 128, hw * 8192, 0); PIN(); }
  }
  if (wave >= 4) {
    const __amdgpu_buffer_rsrc_t rq = mkrs(wq + qs * 16, (DM - 1) * (NH * DH * 2) + 32);
    kb[0] = LD128(rx, lane < 56 ? lane * 16 : (int)OOB, hw * (DM * 2), 0);
    kb[5] = LD128(rws, 0, WS_EPOCH, 0);
#pragma unroll
    for (int i = 0; i < 4; ++i) kb[1 + i] = LD128(rq, (lane >> 1) * (NH * DH * 2) + (lane & 1) * 16, (hw + 4 * i) * 32 * (NH * DH * 2), 0);
    if (wave == 4 && lane >= 8 && lane < 32) misc[lane] = 0u;
  }
  BARRIER();
  const unsigned tstart = now32() + PACE_OFF + (((blk >> 3) * 54) >> 5);
  if (wave < 4) {
#pragma unroll
    for (int i = 0; i < WIN; ++i) kb[i] = LD128(rk, voff, ROFF(i, uE, uO), AUX_NT);
  } else {
#pragma unroll
    for (int i = 6; i < WIN; ++i) asm volatile("" : "=v"(kb[i]));
  }
  asm volatile("" : "+s"(wave));

  const int unit = blk & 63;
  if (wave < 4) {
    const __amdgpu_buffer_rsrc_t rv = mkrs(Vc + (size_t)reg * (4096 * DH), 4096 * DH * 2);
    int shift = 0;
#define PACE_AT(row) do { \
      unsigned t_ = now32(); const unsigned r_ = (unsigned)(row); \
      { const int tg_ = (int)__builtin_amdgcn_readfirstlane(misc[M_SHIFT]); shift = max(min(tg_, shift + SLEW), shift - 1); } \
      const unsigned due_ = tstart + (unsigned)shift + ((r_ * PACE) >> 9) + (((r_ < 128u ? r_ : 128u) * PACE_RAMP) >> 7); \
      if (tid == 0) misc[M_PROG] = (r_ << 16) | ((int)(t_ - due_) > 0 ? min(t_ - due_, 1023u) : 0u); \
      SPIN(64) { if ((int)(t_ - due_) >= 0) break; __builtin_amdgcn_s_sleep(1); t_ = now32(); } } while (0)
#pragma unroll
    for (int i = WIN; i < PRE - 8; ++i) {
      if (((i - WIN) & 3) == 0) PACE_AT(i - WIN);
      USE(kb[i - WIN]); kb[i] = LD128(rk, voff, ROFF(i, uE, uO), AUX_NT); PIN();
    }
    LDS_WAIT_GE(M_QOK, 1, 0);
    const v4u qv = *(const v4u*)(smem + L_QB + c32 * 16);
    const unsigned tag = __builtin_amdgcn_readfirstlane(misc[M_TAG]);
    float* scw = (float*)(smem + L_SC) + (wave * 2 + half) * 64 + (((c32 >> 4) & 1) | ((c32 >> 2) & 2) | (c32 & 4) | ((c32 << 2) & 8));
    const float* pvr = (const float*)(smem + L_PV) + (wave * 2 + half) * 64;
    const bool b3 = c32 & 8, b2 = c32 & 4, b1 = c32 & 2;
    float t0_, t1_, t2_, t3_;
#define KSTEP(v, i, grp) do { \
      float d_ = dot2((v)[0], qv[0], 0.f); d_ = dot2((v)[1], qv[1], d_); d_ = dot2((v)[2], qv[2], d_); d_ = dot2((v)[3], qv[3], d_); \
      if (((i) & 1) == 0) t0_ = d_; else { d_ = swap16_add(t0_, d_); \
        if (((i) & 2) == 0) t1_ = d_; else { d_ = comb<ROR8>(b3, t1_, d_); \
          if (((i) & 4) == 0) t2_ = d_; else { d_ = comb<XOR7>(b2, t2_, d_); \
            if (((i) & 8) == 0) t3_ = d_; else { d_ = comb<XOR2>(b1, t3_, d_); d_ += dpp<XOR1>(d_); \
              scw[((grp) >> 2) * 512 + ((grp) & 3) * 16] = rbf(d_); } } } } \
      asm volatile("" : "+v"(t0_), "+v"(t1_), "+v"(t2_), "+v"(t3_)); } while (0)
    asm volatile("" : "=v"(t0_), "=v"(t1_), "=v"(t2_), "=v"(t3_));
#pragma unroll
    for (int i = 0; i < PRE; ++i) {
      REDEF(kb[i]);
      KSTEP(kb[i], i & 15, i >> 4);
      if (i % (PRE / 16) == 0) {
        const int n = PRE - 8 + i / (PRE / 16);
        if (((n - WIN) & 3) == 0) PACE_AT(n - WIN);
        if (n - WIN > i) USE(kb[n - WIN]);
        PIN(); kb[n % PRE] = LD128(rk, voff, ROFF(n, uE, uO), AUX_NT); PIN();
      }
    }
#pragma unroll 1
    for (int gi = PRE / 16; gi < 16; ++gi) {
      const int cE = gi * 131072 + uE, cO = gi * 131072 + uO;
      const __amdgpu_buffer_rsrc_t rn = gi < 15 ? rk : rv;
      const int nE = gi < 15 ? cE + 131072 : uE, nO = gi < 15 ? cO + 131072 : uO;
#pragma unroll
      for (int i = 0; i < 16; ++i) {
        if ((i & 3) == 0) PACE_AT(gi * 16 + i);
        USE(kb[i]);
        if (i + WIN < 16) kb[i + WIN] = LD128(rk, voff, ROFF(i + WIN, cE, cO), AUX_NT);
        else kb[i + WIN - 16] = LD128(rn, voff, ROFF(i + WIN - 16, nE, nO), AUX_NT);
        REDEF(kb[i]);
        KSTEP(kb[i], i, gi);
      }
      if ((gi & 11) == 11) { PIN(); if (lane == 0) LDS_INC(M_CNT_S + ((gi >> 2) & 1)); }
    }
    float acc[8];
#pragma unroll
    for (int k = 0; k < 8; ++k) acc[k] = 0.f;
    const __amdgpu_buffer_rsrc_t rf = mkrs(Vc + (size_t)bh * (SK * DH), SK * DH * 2);
    const int foff = wave * 4096 + (lane >> 3) * 512 + ds * 128 + (lane & 7) * 16;
#define FOFF(i) (((i) / (FIN / 2) + 1) * 2097152 - FIN * 8192 + ((i) % (FIN / 2)) * 16384)
#define VFMA(i, p) do { REDEF(kb[i]); \
        _Pragma("unroll") for (int k = 0; k < 4; ++k) { acc[2 * k] = fmaf(p, blo(kb[i][k]), acc[2 * k]); acc[2 * k + 1] = fmaf(p, bhi(kb[i][k]), acc[2 * k + 1]); } \
        asm volatile("" : "+v"(acc[0]), "+v"(acc[1]), "+v"(acc[2]), "+v"(acc[3]), "+v"(acc[4]), "+v"(acc[5]), "+v"(acc[6]), "+v"(acc[7])); } while (0)
#define VBODY(gi, LAST) do { \
      f32x4 p4[4]; \
      _Pragma("unroll") for (int i = 0; i < 4; ++i) p4[i] = *(const f32x4*)(pvr + ((gi) >> 2) * 512 + ((gi) & 3) * 16 + i * 4); \
      const int cE = (gi) * 131072 + uE, cO = (gi) * 131072 + uO, nE = cE + 131072, nO = cO + 131072; \
      _Pragma("unroll") for (int i = 0; i < 16; ++i) { \
        if ((i & 3) == 0) PACE_AT(256 + (gi) * 16 + i); \
        USE(kb[i]); \
        if (i + WIN < 16) kb[i + WIN] = LD128(rv, voff, ROFF(i + WIN, cE, cO), AUX_NT); \
        else if (!(LAST)) kb[i + WIN - 16] = LD128(rv, voff, ROFF(i + WIN - 16, nE, nO), AUX_NT); \
        else kb[i + WIN - 16] = LD128(rf, foff, FOFF(i + WIN - 16), AUX_NT); \
        VFMA(i, p4[i >> 2][i & 3]); \
      } } while (0)
    LDS_WAIT_GE(M_P1, 1, 1);
#pragma unroll 1
    for (int gi = 0; gi < 14; ++gi) {
      if (gi == 8 && tid == 0) misc[M_MID] = 1u;
      if (gi == 12) {
        LDS_WAIT_GE(M_P2, 1, 1);
        const float alpha = u2f(misc[M_ALPHA]);
#pragma unroll
        for (int k = 0; k < 8; ++k) acc[k] *= alpha;
      }
      VBODY(gi, false);
    }
    VBODY(14, true);
    {
      f32x4 o;
#pragma unroll
      for (int k = 0; k < 4; ++k) o[k] = swap32_add(acc[k], acc[k + 4]);
      f32x4* oa = (f32x4*)(smem + L_OA) + lane;
      oa[wave * 64] = o;
      PIN();
      unsigned old = 0;
      if (lane == 0) old = LDS_INC(M_CNT_O);
      if (__builtin_amdgcn_readfirstlane(old) == 3u) {
        PIN();
        o = (oa[0] + oa[64]) + (oa[128] + oa[192]);
        ST128(enc4(o, tag), rws, (c32 >> 3) * 1024 + (c32 & 7) * 32 + half * 16, WS_OX + bh * 4096 + sp * 256, AUX_SC1);
        if (lane == 0) misc[M_OPUB] = 1u;
      }
    }
    {
      LDS_WAIT_GE(M_PFOK, 1, 1);
      f32x4 p4[FIN / 4];
#pragma unroll
      for (int i = 0; i < FIN / 4; ++i) p4[i] = *((const f32x4*)(smem + L_PF) + (wave * 8 + (lane >> 3)) * (FIN / 4) + i);
#pragma unroll
      for (int k = 0; k < 8; ++k) acc[k] = 0.f;
#pragma unroll
      for (int i = 16 - FIN; i < 16; ++i) {
        if ((i & 3) == 0 && i < 8) PACE_AT(496 + i);
        USE(kb[i]);
        if (i + WIN < 16) kb[i + WIN] = LD128(rf, foff, FOFF(i + WIN), AUX_NT);
        VFMA(i, p4[(i - 16 + FIN) >> 2][i & 3]);
      }
      const float s0 = swap32_add(acc[0], acc[4]), s1 = swap32_add(acc[1], acc[5]), s2 = swap32_add(acc[2], acc[6]), s3 = swap32_add(acc[3], acc[7]);
      float u0 = swap16_add(s0, s2), u1 = swap16_add(s1, s3);
      u0 += dpp<ROR8>(u0); u1 += dpp<ROR8>(u1);
      *(v2u*)(smem + L_FN + wave * 256 + (lane & 7) * 32 + half * 16 + (g4 & 1) * 8) = v2u{f2u(u0), f2u(u1)};
      PIN();
      if (lane == 0) LDS_INC(M_CNT_FN);
    }
    const int ru = unit - RU0;
    if (ru < 0 || ru >= NUNIT) return;
    const v2u xr = LD64(rx, (lane & 7) * 8, b * (DM * 2) + ru * 64, 0);
    const int po = (lane >> 3) * (DM * 4) + (lane & 7) * 16;
    float* rs = (float*)(smem + L_RS);
    LDS_WAIT_GE(M_PDONE, 1, 0);
#pragma unroll 1
    for (int ph = 0; ph < 2; ++ph) {
      const unsigned ib = (ph == 0 ? WS_Y + (unsigned)(b * 64 + wave * 16) * (DM * 4u) : WS_O + (unsigned)(b * NM + wave * 28) * (DM * 4u)) + ru * 128;
      const bool skip2 = ph == 0, skip3 = ph == 0 || lane >= 32;
      const int po2 = skip2 ? (int)OOB : po, po3 = skip3 ? (int)OOB : po;
      v4u p0, p1, p2, p3;
      SPIN(SPIN_CAP) {
        PIN();
        p0 = LD128(rws, po, ib, AUX_SC1); p1 = LD128(rws, po, ib + 8 * DM * 4, AUX_SC1);
        p2 = LD128(rws, po2, ib + 16 * DM * 4, AUX_SC1); p3 = LD128(rws, po3, ib + 24 * DM * 4, AUX_SC1);
        const bool ok = valid4(p0, tag) & valid4(p1, tag) & (valid4(p2, tag) | skip2) & (valid4(p3, tag) | skip3);
        if (__all(ok)) break;
      }
      const f32x4 v = (dec4(p0) + dec4(p1)) + (dec4(p2) + dec4(p3));
      float c = swap16_add(swap32_add(v[0], v[2]), swap32_add(v[1], v[3]));
      c += dpp<ROR8>(c);
      rs[wave * 32 + (lane & 7) * 4 + 2 * half + (g4 & 1)] = c;
      PIN();
      unsigned old = 0;
      if (lane == 0) old = LDS_INC(M_CNT_R);
      if (__builtin_amdgcn_readfirstlane(old) == 3u + 4u * ph) {
        PIN();
        const f32x4* r4 = (const f32x4*)rs + (lane & 7);
        const f32x4 sum = (r4[0] + r4[8]) + (r4[16] + r4[24]);
        if (ph == 0) {
          f32x4 res;
          res[0] = rbf(blo(xr[0]) + rbf(sum[0])); res[1] = rbf(bhi(xr[0]) + rbf(sum[1])); res[2] = rbf(blo(xr[1]) + rbf(sum[2])); res[3] = rbf(bhi(xr[1]) + rbf(sum[3]));
          ST128(enc4(res, tag), rws, (lane >> 3) * (NB * DM * 4) + (lane & 7) * 16, WS_HB + b * (DM * 4) + ru * 128, AUX_SC1);
          *((f32x4*)rs + 32 + (lane & 7)) = res;
          PIN();
          if (lane == 0) misc[M_RDONE] = 1u;
        } else {
          const f32x4 hq = r4[32];
          const v2u ov = {pk2(hq[0] + rbf(sum[0]), hq[1] + rbf(sum[1])), pk2(hq[2] + rbf(sum[2]), hq[3] + rbf(sum[3]))};
          ST64(ov, mkrs(out, NB * DM * 2), lane < 8 ? lane * 8 : (int)OOB, b * (DM * 2) + ru * 64, 0);
          if (blk == RU0) {
            ST32(misc[M_EP], rws, lane == 0 ? 0 : (int)OOB, WS_EPOCH, AUX_SC1);
          }
        }
      }
      if (ph == 0) LDS_WAIT_GE(M_RDONE, 1, 1);
    }
    asm volatile(".Lfd_code_end2:");
    return;
  }
  const bool is_m = unit < 28;
  const int mi = unit * 4 + b;
  const int tq = c16 >> 2, tp = c16 & 3;
  const LAS unsigned char* s1 = (const LAS unsigned char*)(smem + L_W1) + (g4 * 8 + tq) * 32 + tp * 8;
  const unsigned char* hp = smem + L_HN + (c16 & 3) * HN_STRIDE + g4 * 16;
  {
    float v[8];
#pragma unroll
    for (int k = 0; k < 4; ++k) { v[2 * k] = blo(kb[0][k]); v[2 * k + 1] = bhi(kb[0][k]); }
    ln8(v, lane);
    const v4u xn = {pk2(v[0], v[1]), pk2(v[2], v[3]), pk2(v[4], v[5]), pk2(v[6], v[7])};
    if (lane < 56) *(v4u*)(smem + L_HN + hw * HN_STRIDE + lane * 16) = xn;
#pragma unroll
    for (int i = 0; i < 4; ++i) if (hw + 4 * i < 14) *(v4u*)(smem + L_W1 + (hw + 4 * i) * 1024 + lane * 16) = kb[1 + i];
    PIN();
    if (lane == 0) LDS_INC(M_CNT_Q);
  }
  const unsigned e0 = __builtin_amdgcn_readfirstlane(kb[5][0]);
  USE(cpf);
  unsigned qtag = (e0 >> 8) + 0x9E3779B9u;
  qtag ^= qtag >> 16; qtag *= 0x85EBCA6Bu; qtag ^= qtag >> 13; qtag *= 0xC2B2AE35u; qtag ^= qtag >> 16;
  qtag ^= (e0 & 255u) * 0x01010101u;
  if (hw == 0) {
    if (lane == 0) { misc[M_TAG] = qtag; misc[M_EP] = e0 + 1u; }
    LDS_WAIT_GE(M_CNT_Q, 4, 0);
    bf16x8 a1[14], bfr[14];
#pragma unroll
    for (int s = 0; s < 14; ++s) {
      a1[s] = __builtin_shufflevector(trd(s1 + s * 1024), trd(s1 + s * 1024 + 128), 0, 1, 2, 3, 4, 5, 6, 7);
      bfr[s] = *(const bf16x8*)(hp + s * 64);
    }
    __builtin_amdgcn_sched_barrier(0);
    f32x4 da = {0.f, 0.f, 0.f, 0.f}, db = {0.f, 0.f, 0.f, 0.f};
#pragma unroll
    for (int s = 0; s < 14; s += 2) {
      da = __builtin_amdgcn_mfma_f32_16x16x32_bf16(a1[s], bfr[s], da, 0, 0, 0);
      db = __builtin_amdgcn_mfma_f32_16x16x32_bf16(a1[s + 1], bfr[s + 1], db, 0, 0, 0);
    }
    da += db;
    const v4u qg = {pk2(da[0], da[1]), qtag, pk2(da[2], da[3]), qtag};
    ST128(qg, rws, c16 < 4 ? c16 * 64 + g4 * 16 : (int)OOB, WS_Q + qs * 256, AUX_SC1);
    if (lane == 0) LDS_INC(M_CNT_Q);
  } else if (hw == 1) {
    v4u qg;
    LDS_WAIT_GE(M_CNT_Q, 5, 1);
    SPIN(SPIN_CAP) {
      PIN();
      qg = LD128(rws, (lane >> 2) * 256 + (lane & 3) * 16, WS_Q + h * 4096 + b * 64, AUX_SC1);
      if (__all(qg[1] == qtag && qg[3] == qtag)) break;
    }
    *(v2u*)(smem + L_QB + lane * 8) = v2u{qg[0], qg[2]};
    PIN();
    if (lane == 0) misc[M_QOK] = 1u;
  }
  LDS_WAIT_GE(M_QOK, 1, 2);
  bf16x8 a0[16];
  {
    const __amdgpu_buffer_rsrc_t r0 = mkrs(wo + (size_t)(h * DH + ds * 64) * DM, 64 * DM * 2);
    if (hw < 2) {
#pragma unroll 1
      for (int i = 0; i < 7; ++i) {
        __builtin_amdgcn_s_sleep(100);
        PIN();
        v4u tw[4];
#pragma unroll
        for (int k = 0; k < 4; ++k) tw[k] = LD128(r0, lane * 16, (hw + 8 * i + 2 * k) * 1024, 0);
#pragma unroll
        for (int k = 0; k < 4; ++k) *(v4u*)(smem + L_WO + (hw + 8 * i + 2 * k) * 1024 + lane * 16) = tw[k];
      }
    } else if (hw == 2) {
      if ((blk & 0x78) == 0) {
#pragma unroll 1
        for (unsigned c = 1; c < NCHK; ++c) {
          LDS_WAIT_GE(M_PROG, (c * PER + CH0) << 16, 4);
          ST32((qtag & ~1023u) | (misc[M_PROG] & 1023u), rws, lane == 0 ? 0 : (int)OOB, WS_LAG + c * 2048 + ((blk >> 4) + (blk & 7)) * 128, AUX_SC1);
        }
      }
    } else {
      unsigned target = 0;
#pragma unroll 1
      for (unsigned c = 1; c < NCHK; ++c) {
        LDS_WAIT_GE(M_PROG, (c * PER + CH0 + RDROWS) << 16, 8);
        const unsigned v = __builtin_amdgcn_raw_buffer_load_b32(rws, c16 * 128, WS_LAG + c * 2048, AUX_SC1);
        float lag = (v ^ qtag) < 1024u ? (float)(v & 1023u) : 150.f;
        lag = row_max(lag);
        const unsigned lg = __builtin_amdgcn_readfirstlane((unsigned)lag);
        target += lg > DEAD ? min((lg - DEAD) >> 2, INCMAX) : lg < LOWL ? 0u - BONUS : 0u;
        if (lane == 0) misc[M_SHIFT] = target;
      }
    }
    const unsigned d1 = __builtin_amdgcn_raw_buffer_load_b16(mkrs(hw == 0 ? w1 : hw == 1 ? w2 : hw == 2 ? w1 + DM * FF - 64 : w2 + DM * FF - 64, 64), 0, 0, 0);
    const unsigned d3 = __builtin_amdgcn_raw_buffer_load_b16(mkrs(Vc + (size_t)(reg ^ (hw & 1)) * (4096 * DH) + 4096 * DH / 2, 64), 0, 0, AUX_NT);
    USE(d1); USE(d3);
    if (lane == 0) LDS_INC(M_CNT_W);
  }
  if (hw == 0) {
    const f32x4* scl = (const f32x4*)(smem + L_SC) + lane;
    f32x4* pvl = (f32x4*)(smem + L_PV) + lane;
    f32x4 sv[6];
    LDS_WAIT_GE(M_CNT_S, 4, 8);
#pragma unroll
    for (int i = 0; i < 6; ++i) sv[i] = scl[i * 64];
    float m1 = -INFINITY;
#pragma unroll
    for (int i = 0; i < 6; ++i) m1 = fmaxf(m1, fmaxf(fmaxf(sv[i][0], sv[i][1]), fmaxf(sv[i][2], sv[i][3])));
    m1 = wave_max(m1);
    float l = 0.f;
#pragma unroll
    for (int i = 0; i < 6; ++i) {
#pragma unroll
      for (int e = 0; e < 4; ++e) { sv[i][e] = __builtin_amdgcn_exp2f((sv[i][e] - m1) * SCL2E); l += sv[i][e]; }
      pvl[i * 64] = sv[i];
    }
    PIN();
    if (lane == 0) misc[M_P1] = 1u;
    LDS_WAIT_GE(M_CNT_S2, 4, 8);
    sv[0] = scl[384]; sv[1] = scl[448];
    f32x4 pub = *((const f32x4*)(smem + L_SC) + 384 + ((lane / (FIN / 4)) & 7) * 16 + 16 - FIN / 4 + (lane % (FIN / 4)));
    float m = m1;
#pragma unroll
    for (int i = 0; i < 2; ++i) m = fmaxf(m, fmaxf(fmaxf(sv[i][0], sv[i][1]), fmaxf(sv[i][2], sv[i][3])));
    m = wave_max(m);
    const float alpha = __builtin_amdgcn_exp2f((m1 - m) * SCL2E);
    l *= alpha;
#pragma unroll
    for (int i = 0; i < 2; ++i) {
#pragma unroll
      for (int e = 0; e < 4; ++e) { sv[i][e] = __builtin_amdgcn_exp2f((sv[i][e] - m) * SCL2E); l += sv[i][e]; }
      pvl[384 + i * 64] = sv[i];
    }
    l = wave_sum(l);
    if (lane == 0) misc[M_ALPHA] = f2u(alpha);
    PIN();
    if (lane == 0) misc[M_P2] = 1u;
    if (lane == 32) { pub[0] = m; pub[1] = l; }
    ST128(enc4(pub, qtag), rws, lane < 2 * FIN ? (int)(WS_SX + (bh * 4 + sp) * (FIN * 32)) + lane * 16 : lane == 32 ? (int)(WS_ML + bh * 128 + sp * 16) : (int)OOB, 0, AUX_SC1);
  }
  LDS_WAIT_GE(M_MID, 1, 16);
  LDS_WAIT_GE(M_CNT_W, 4, 2);
  {
    const LAS unsigned char* s0 = (const LAS unsigned char*)(smem + L_WO) + (g4 * 8 + tq) * (DM * 2) + (hw * 64 + tp * 4) * 2;
#pragma unroll
    for (int t = 0; t < 16; ++t) {
      const LAS unsigned char* p = s0 + (t & 3) * 32 + ((t >> 2) & 1) * 512 + (t >> 3) * (32 * DM * 2);
      a0[t] = __builtin_shufflevector(trd(p), trd(p + 4 * DM * 2), 0, 1, 2, 3, 4, 5, 6, 7);
    }
#pragma unroll
    for (int t = 0; t < 16; ++t) asm volatile("" : "+v"(a0[t]));
  }
  float wn;
  {
    v4u pm;
    SPIN(SPIN_CAP) {
      PIN();
      pm = LD128(rws, (lane & 3) * 16, WS_ML + bh * 128, AUX_SC1);
      if (__all(valid4(pm, qtag))) break;
      __builtin_amdgcn_s_sleep(32);
    }
    const float ms = u2f(pm[0] & 0xFFFFFF00u), ls = u2f((pm[1] & 0xFFFFFF00u) | 0x80u);
    float M = fmaxf(ms, dpp<XOR1>(ms)); M = fmaxf(M, dpp<XOR2>(M));
    const float e = __builtin_amdgcn_exp2f((ms - M) * SCL2E);
    float L = ls * e; L += dpp<XOR1>(L); L += dpp<XOR2>(L);
    const float rl = __builtin_amdgcn_rcpf(L);
    wn = e * rl;
    if (hw == 1) {
      v4u sa, sb;
      SPIN(SPIN_CAP) {
        PIN();
        sa = LD128(rws, lane * 16, WS_SX + bh * 2048, AUX_SC1); sb = LD128(rws, lane * 16, WS_SX + bh * 2048 + 1024, AUX_SC1);
        if (__all(valid4(sa, qtag) & valid4(sb, qtag))) break;
        __builtin_amdgcn_s_sleep(32);
      }
      float* pf = (float*)(smem + L_PF) + ((lane >> 2) & 7) * 16 + (lane & 3) * 2;
      const f32x4 fa = dec4x(sa), fb = dec4x(sb);
#pragma unroll
      for (int e4 = 0; e4 < 4; ++e4) {
        const int w = ((half ^ (e4 >> 1)) << 1) | (e4 & 1);
        pf[w * 128 + (e4 >> 1)] = __builtin_amdgcn_exp2f((fa[e4] - M) * SCL2E) * rl;
        pf[w * 128 + 8 + (e4 >> 1)] = __builtin_amdgcn_exp2f((fb[e4] - M) * SCL2E) * rl;
      }
      PIN();
      if (lane == 0) misc[M_PFOK] = 1u;
    }
  }
  v4u t1[4], t2[4];
  {
    const int dof = ((c16 >> 3) * 32 + g4 * 8 + ((c16 >> 2) & 1) * 4) * 4;
    LDS_WAIT_GE(M_OPUB, 1, 2);
    { const unsigned tt = now32() + P_OWN; SPIN(512) { if ((int)(now32() - tt) >= 0) break; } }
    v4u pv;
    SPIN(SPIN_CAP) {
      PIN();
      pv = LD128(rws, (c16 & 3) * 256 + dof, WS_OX + bh * 4096 + ds * 1024, AUX_SC1);
      if (__all(valid4(pv, qtag))) break;
    }
    f32x4 o = dec4(pv) * wn;
    LDS_WAIT_GE(M_CNT_FN, 4, 0);
    o += *(const f32x4*)(smem + L_FN + (c16 & 3) * 256 + dof);
#pragma unroll
    for (int k = 0; k < 4; ++k) { o[k] += dpp<XOR1>(o[k]); o[k] += dpp<XOR2>(o[k]); }
    v4u b0, b1;
    b0[0] = pk2(o[0], o[1]); b0[1] = pk2(o[2], o[3]);
    b0[2] = f2u(dpp<0x104>(u2f(b0[0]))); b0[3] = f2u(dpp<0x104>(u2f(b0[1])));
    b1[0] = f2u(dpp<0x108>(u2f(b0[0]))); b1[1] = f2u(dpp<0x108>(u2f(b0[1])));
    b1[2] = f2u(dpp<0x10C>(u2f(b0[0]))); b1[3] = f2u(dpp<0x10C>(u2f(b0[1])));
    f32x4 y[8];
#pragma unroll
    for (int t = 0; t < 8; ++t) y[t] = __builtin_amdgcn_mfma_f32_16x16x32_bf16(a0[t], __builtin_bit_cast(bf16x8, b0), f32x4{0.f, 0.f, 0.f, 0.f}, 0, 0, 0);
#pragma unroll
    for (int t = 0; t < 8; ++t) y[t] = __builtin_amdgcn_mfma_f32_16x16x32_bf16(a0[8 + t], __builtin_bit_cast(bf16x8, b1), y[t], 0, 0, 0);
    __builtin_amdgcn_sched_barrier(0);
    const int vo = c16 < 4 ? c16 * 64 + g4 * 16 : (int)OOB;
    const bool s1b = c16 & 1, s2b = c16 & 2;
    const unsigned yb = WS_Y + (unsigned)(bh * 4 + ds) * (DM * 4u) + hw * 256;
    ST128(enc4(sel4(s1b, s2b, y[0], y[1], y[2], y[3]), qtag), rws, vo, yb, AUX_SC1);
    ST128(enc4(sel4(s1b, s2b, y[4], y[5], y[6], y[7]), qtag), rws, hw < 3 ? vo : (int)OOB, yb + 1024, AUX_SC1);
    if (lane == 0) misc[M_PDONE] = 1u;
    {
      const __amdgpu_buffer_rsrc_t r1 = mkrs(w1 + mi * 16, is_m ? (DM - 1) * (FF * 2) + 32 : 0);
      const __amdgpu_buffer_rsrc_t r2 = mkrs(w2 + (size_t)mi * 16 * DM, is_m ? 16 * DM * 2 : 0);
#pragma unroll
      for (int i = 0; i < 4; ++i) {
        t1[i] = LD128(r1, (lane >> 1) * (FF * 2) + (lane & 1) * 16, (hw + 4 * i) * 32 * (FF * 2), 0);
        t2[i] = LD128(r2, lane * 16, (hw + 4 * i) * 1024, 0);
      }
      PIN();
    }
  }
  if (!is_m) return;
  bf16x8 a1[14];
  s16x4 a2[8];
  {
#pragma unroll
    for (int i = 0; i < 4; ++i) if (hw + 4 * i < 14) {
      *(v4u*)(smem + L_W1 + (hw + 4 * i) * 1024 + lane * 16) = t1[i];
      *(v4u*)(smem + L_W2 + (hw + 4 * i) * 1024 + lane * 16) = t2[i];
    }
    PIN();
    if (lane == 0) LDS_INC(M_CNT_W);
    LDS_WAIT_GE(M_CNT_W, 8, 0);
    const LAS unsigned char* s2 = (const LAS unsigned char*)(smem + L_W2) + (g4 * 4 + tq) * (DM * 2) + (hw * 64 + tp * 4) * 2;
#pragma unroll
    for (int t = 0; t < 8; ++t) a2[t] = __builtin_bit_cast(s16x4, trd(s2 + (t & 3) * 32 + (t >> 2) * 512));
#pragma unroll
    for (int s = 0; s < 14; ++s) a1[s] = __builtin_shufflevector(trd(s1 + s * 1024), trd(s1 + s * 1024 + 128), 0, 1, 2, 3, 4, 5, 6, 7);
#pragma unroll
    for (int t = 0; t < 7; ++t) asm volatile("" : "+v"(a2[t]), "+v"(a1[2 * t]), "+v"(a1[2 * t + 1]));
    asm volatile("" : "+v"(a2[7]));
  }
  v4u h0, h1;
  {
    const unsigned hb = WS_HB + (unsigned)(blk & 7) * (NB * DM * 4) + hw * (DM * 4);
    const int vo1 = lane < 48 ? lane * 16 : (int)OOB;
    SPIN(SPIN_CAP) {
      PIN();
      h0 = LD128(rws, lane * 16, hb, AUX_SC1); h1 = LD128(rws, vo1, hb + 1024, AUX_SC1);
      if (__all(valid4(h0, qtag) & (valid4(h1, qtag) | (lane >= 48)))) break;
    }
  }
  {
    float v[8];
#pragma unroll
    for (int k = 0; k < 4; ++k) { v[k] = bhi(h0[k]); v[k + 4] = bhi(h1[k]); }
    ln8b(v, lane);
    *(v2u*)(smem + L_HN + hw * HN_STRIDE + lane * 8) = v2u{pk2(v[0], v[1]), pk2(v[2], v[3])};
    if (lane < 48) *(v2u*)(smem + L_HN + hw * HN_STRIDE + 512 + lane * 8) = v2u{pk2(v[4], v[5]), pk2(v[6], v[7])};
    PIN();
    if (lane == 0) LDS_INC(M_CNT_HN);
    LDS_WAIT_GE(M_CNT_HN, 4, 0);
  }
  f32x4 d1a = {0.f, 0.f, 0.f, 0.f}, d1b = {0.f, 0.f, 0.f, 0.f};
  {
    bf16x8 bfr[14];
#pragma unroll
    for (int s = 0; s < 14; ++s) bfr[s] = *(const bf16x8*)(hp + s * 64);
    __builtin_amdgcn_sched_barrier(0);
#pragma unroll
    for (int s = 0; s < 14; s += 2) {
      d1a = __builtin_amdgcn_mfma_f32_16x16x32_bf16(a1[s], bfr[s], d1a, 0, 0, 0);
      d1b = __builtin_amdgcn_mfma_f32_16x16x32_bf16(a1[s + 1], bfr[s + 1], d1b, 0, 0, 0);
    }
  }
  float sv[4];
#pragma unroll
  for (int r = 0; r < 4; ++r) { const float t = rbf(d1a[r] + d1b[r]); sv[r] = t * __builtin_amdgcn_rcpf(1.0f + __builtin_amdgcn_exp2f(-t * LOG2E)); }
  const v2u hbp = {pk2(sv[0], sv[1]), pk2(sv[2], sv[3])};
  const s16x4 b2v = __builtin_bit_cast(s16x4, hbp);
  const int vo = (c16 & 3) * (NM * DM * 4) + (c16 >> 2) * 64 + g4 * 16;
  f32x4 o2[8];
#pragma unroll
  for (int t = 0; t < 8; ++t) o2[t] = __builtin_amdgcn_mfma_f32_16x16x16bf16_1k(a2[t], b2v, f32x4{0.f, 0.f, 0.f, 0.f}, 0, 0, 0);
  __builtin_amdgcn_sched_barrier(0);
  {
    const bool s1b = c16 & 4, s2b = c16 & 8;
    const unsigned ob = WS_O + (unsigned)mi * (DM * 4u) + hw * 256;
    ST128(enc4(sel4(s1b, s2b, o2[0], o2[1], o2[2], o2[3]), qtag), rws, vo, ob, AUX_SC1);
    ST128(enc4(sel4(s1b, s2b, o2[4], o2[5], o2[6], o2[7]), qtag), rws, hw < 3 ? vo : (int)OOB, ob + 1024, AUX_SC1);
  }
  asm volatile(".Lfd_code_end:");
}

extern "C" void kernel_launch(void* const* d_in, const int* in_sizes, int n_in, void* d_out, int out_size, void* d_ws, size_t ws_size, hipStream_t stream) {
  fused_decode<<<NWG, NTHR, 0, stream>>>((const u16*)d_in[0], (const u16*)d_in[1], (const u16*)d_in[2], (const u16*)d_in[3], (const u16*)d_in[4],
                                         (const u16*)d_in[6], (const u16*)d_in[7], (u16*)d_out, (unsigned char*)d_ws);
}
```
